```python
import torch

def _softmax(x, axis=-1):
    return torch.softmax(x, dim=axis)
def _gelu(x):
    return torch.nn.functional.gelu(x, approximate="tanh")
def _silu(x):
    return torch.nn.functional.silu(x)
def _sigmoid(x):
    return torch.sigmoid(x)
def _layernorm(x, axis=-1, eps=1e-5):
    m = x.mean(dim=axis, keepdim=True)
    v = x.var(dim=axis, keepdim=True, unbiased=False)
    return (x - m) / torch.sqrt(v + eps)
def _scatter_add(tgt, idx, src, axis=0):
    return tgt.clone().index_add_(axis, idx, src)
def _segment_sum(src, seg, n):
    out = torch.zeros((n,) + src.shape[1:], dtype=src.dtype)
    return out.index_add_(0, seg, src)
def _pad(x, pw):
    flat = tuple(v for pair in reversed(pw) for v in pair)
    return torch.nn.functional.pad(x, flat)

kernel_name = 'interleave_layers__sliding_local_dilated__moe_arch_mod__prompt'

def setup_inputs():

    _w = (lambda a: (lambda t: t.to(torch.bfloat16) if t.is_floating_point() else t)(torch.as_tensor(a)))
    def _A_setup():
        B, S, D = 1, 2048, 512
        return {
            "x": _w((lambda *s: torch.randn(*s, dtype=torch.bfloat16) * 0.05)(B, S, D)),
            "wqkv": _w((lambda *s: torch.randn(*s, dtype=torch.bfloat16) * 0.05)(D, 3 * D)),
            "wo": _w((lambda *s: torch.randn(*s, dtype=torch.bfloat16) * 0.05)(D, D)),
        }
    _A_in = _A_setup()

    def _B_setup():
        B, S, D, F, C = 1, 2048, 512, 1792, 8
        return {
            "x": _w((lambda *s: torch.randn(*s, dtype=torch.bfloat16) * 0.05)(B, S, D)),
            "w_r": _w((lambda *s: torch.randn(*s, dtype=torch.bfloat16) * 0.05)(D)),
            "w_gate": _w((lambda *s: torch.randn(*s, dtype=torch.bfloat16) * 0.05)(D, F)),
            "w_up": _w((lambda *s: torch.randn(*s, dtype=torch.bfloat16) * 0.05)(D, F)),
            "w_down": _w((lambda *s: torch.randn(*s, dtype=torch.bfloat16) * 0.05)(F, D)),
        }
    _B_in = _B_setup()

    return {
        "A_x": _A_in["x"],
        "A_wqkv": _A_in["wqkv"],
        "A_wo": _A_in["wo"],
        "B_x": _B_in["x"],
        "B_w_r": _B_in["w_r"],
        "B_w_gate": _B_in["w_gate"],
        "B_w_up": _B_in["w_up"],
        "B_w_down": _B_in["w_down"],
    }

def reference(A_x, A_wqkv, A_wo, B_x, B_w_r, B_w_gate, B_w_up, B_w_down):
    def _A_ref(x, wqkv, wo):
        B, S, D = x.shape
        H = 32; Dh = D // H
        W, dil = 8, 2
        qkv = (lambda x, *d: x.permute(*d))((x @ wqkv).reshape(B, S, 3, H, Dh), 2, 0, 3, 1, 4)
        q, k, v = qkv[0], qkv[1], qkv[2]
        scale = Dh ** -0.5
        pos = torch.arange(S)
        off = torch.arange(W) * dil
        idx = pos[:, None] - off[None, :]
        mask = idx >= 0
        idx_c = torch.where(mask, idx, 0).reshape(-1)
        kg = k[:, :, idx_c, :].reshape(B, H, S, W, Dh)
        vg = v[:, :, idx_c, :].reshape(B, H, S, W, Dh)
        logits = torch.einsum("bhsd,bhswd->bhsw", q, kg) * scale
        logits = torch.where(mask[None, None, :, :], logits, -1e9)
        attn = _softmax(logits, axis=-1)
        o = torch.einsum("bhsw,bhswd->bhsd", attn, vg)
        return (lambda x, *d: x.permute(*d))(o, 0, 2, 1, 3).reshape(B, S, D) @ wo

    def _B_ref(x, w_r, w_gate, w_up, w_down):
        B, S, D = x.shape
        C = 8
        r = x @ w_r
        idx = torch.argsort(-r, axis=-1)[:, :C]
        idx_e = (lambda x, n, axis: torch.repeat_interleave(x, n, dim=axis))(idx.reshape(B, C, 1), D, axis=2)
        x_sel = (lambda x, i, axis: torch.take_along_dim(x, i, dim=axis))(x, idx_e, axis=1)
        g = _silu(x_sel @ w_gate)
        u = x_sel @ w_up
        y = (g * u) @ w_down
        r_sel = _sigmoid((lambda x, i, axis: torch.take_along_dim(x, i, dim=axis))(r, idx, axis=1))
        y = y * r_sel.reshape(B, C, 1)
        b_off = (torch.arange(B) * S).reshape(B, 1)
        flat_idx = (idx + b_off).reshape(B * C)
        zero = (x * 0.0).reshape(B * S, D)
        upd = _scatter_add(zero, flat_idx, y.reshape(B * C, D), axis=0)
        return x + upd.reshape(B, S, D)

    x = A_x
    x = x + _A_ref(_layernorm(x, axis=-1), A_wqkv, A_wo)
    x = x + _B_ref(_layernorm(x, axis=-1), B_w_r, B_w_gate, B_w_up, B_w_down)
    x = x + _A_ref(_layernorm(x, axis=-1), A_wqkv, A_wo)
    x = x + _B_ref(_layernorm(x, axis=-1), B_w_r, B_w_gate, B_w_up, B_w_down)
    return x
```

```cpp
#include <hip/hip_runtime.h>
#include <hip/hip_bf16.h>
#include <stdint.h>

#define LAS __attribute__((address_space(3)))
#define GAS __attribute__((address_space(1)))
typedef unsigned short u16;
typedef short v4s __attribute__((ext_vector_type(4)));
typedef short v8s __attribute__((ext_vector_type(8)));
typedef __bf16 v2bf __attribute__((ext_vector_type(2)));
typedef float f32x4 __attribute__((ext_vector_type(4)));
typedef float f32x2 __attribute__((ext_vector_type(2)));
typedef unsigned v4u __attribute__((ext_vector_type(4)));
typedef unsigned v2u __attribute__((ext_vector_type(2)));

constexpr int S = 2048, D = 512, F = 1792;
constexpr unsigned SENT = 0xFFFFFFFFu;

__device__ __forceinline__ float bf_lo(unsigned u) { return __uint_as_float(u << 16); }
__device__ __forceinline__ float bf_hi(unsigned u) { return __uint_as_float(u & 0xffff0000u); }
__device__ __forceinline__ unsigned pack2(float a, float b) {
  f32x2 f = {a, b};
  v2bf c = __builtin_convertvector(f, v2bf);
  return __builtin_bit_cast(unsigned, c);
}
__device__ __forceinline__ float rbf(float a) {
  __bf16 c = (__bf16)a;
  return __uint_as_float(((unsigned)__builtin_bit_cast(u16, c)) << 16);
}
__device__ __forceinline__ float dot2(unsigned a, unsigned b, float c) {
  return __builtin_amdgcn_fdot2_f32_bf16(__builtin_bit_cast(v2bf, a), __builtin_bit_cast(v2bf, b), c, false);
}
__device__ __forceinline__ v8s trfrag(const char LAS* p0, int off) {
  v4s a = __builtin_amdgcn_ds_read_tr16_b64_v4i16((LAS v4s*)(p0 + off));
  v4s b = __builtin_amdgcn_ds_read_tr16_b64_v4i16((LAS v4s*)(p0 + off + 256));
  v8s r = {a[0], a[1], a[2], a[3], b[0], b[1], b[2], b[3]};
  return r;
}
__device__ __forceinline__ void glds16(const void* g, void LAS* l) {
  __builtin_amdgcn_global_load_lds((const void GAS*)g, l, 16, 0, 0);
}
__device__ __forceinline__ void ln_scalars(float mean, float var, float& mb, float& rinv) {
  mb = rbf(mean);
  float vb = rbf(fmaxf(var, 0.f));
  float t = rbf(vb + 1e-5f);
  float sg = rbf(sqrtf(t));
  rinv = 1.0f / sg;
}

constexpr int QROW = 208;
constexpr int K1_LDS = 98304 + 144 * QROW + 4 * 96 * 4;
__global__ __launch_bounds__(256, 1) void k_qkv_attn(const u16* __restrict__ X, const u16* __restrict__ W,
                                                     u16* __restrict__ O, unsigned* __restrict__ fill) {
  extern __shared__ __attribute__((aligned(16))) char smem[];
  char LAS* sm = (char LAS*)smem;
  const int tid = threadIdx.x, l = tid & 63, w = __builtin_amdgcn_readfirstlane(tid >> 6);
  const int b = blockIdx.x, xcd = b & 7, j = b >> 3;
  const int t = 4 * (xcd >> 1) + (j >> 3), g = 8 * (xcd & 1) + (j & 7);
  const int t0 = 128 * t;
  fill[b * 256 + tid] = SENT;
  {
    const int row = l >> 2, c16 = (l & 3) ^ (((row >> 3) & 1) << 1);
    const u16* src = W + (size_t)row * 1536 + g * 32 + c16 * 8;
#pragma unroll
    for (int i = 0; i < 24; ++i) {
      const int item = 4 * i + w, kb = item / 3, p = item % 3;
      glds16(src + (size_t)kb * 16 * 1536 + p * 512, sm + p * 32768 + kb * 1024);
    }
  }
  v4u xa[2][16], xh[16];
  {
    const int g4 = l >> 4;
    const u16* p0 = X + (size_t)(t0 + 32 * w + (l & 15)) * D + 8 * g4;
    int hr = t0 - 16 + (l & 15);
    hr = hr < 0 ? 0 : hr;
    const u16* ph = X + (size_t)hr * D + 8 * g4;
#pragma unroll
    for (int s = 0; s < 16; ++s) {
      xa[0][s] = *(const v4u*)(p0 + 32 * s);
      xa[1][s] = *(const v4u*)(p0 + 16 * D + 32 * s);
      xh[s] = *(const v4u*)(ph + 32 * s);
    }
  }
  asm volatile("s_waitcnt vmcnt(0)" ::: "memory");
  __syncthreads();
  const int g4 = l >> 4, q = (l & 15) >> 2, pp = l & 3;
  const char LAS* wb0 = sm + (8 * g4 + q) * 64 + (((pp >> 1) ^ ((g4 & 1) << 1)) * 16) + 8 * (pp & 1);
  const char LAS* wb1 = sm + (8 * g4 + q) * 64 + ((((pp >> 1) ^ ((g4 & 1) << 1)) ^ 2) * 16) + 8 * (pp & 1);
  float s1[3] = {0, 0, 0}, s2[3] = {0, 0, 0};
#pragma unroll
  for (int s = 0; s < 16; ++s)
#pragma unroll
    for (int e = 0; e < 4; ++e) {
      s1[0] = dot2(xa[0][s][e], 0x3f803f80u, s1[0]);
      s2[0] = dot2(xa[0][s][e], xa[0][s][e], s2[0]);
      s1[1] = dot2(xa[1][s][e], 0x3f803f80u, s1[1]);
      s2[1] = dot2(xa[1][s][e], xa[1][s][e], s2[1]);
      s1[2] = dot2(xh[s][e], 0x3f803f80u, s1[2]);
      s2[2] = dot2(xh[s][e], xh[s][e], s2[2]);
    }
  float mb[3], ri[3];
#pragma unroll
  for (int i = 0; i < 3; ++i) {
    float a = s1[i], c = s2[i];
    a += __shfl_xor(a, 16); c += __shfl_xor(c, 16);
    a += __shfl_xor(a, 32); c += __shfl_xor(c, 32);
    const float mean = a * (1.0f / D);
    const float var = c * (1.0f / D) - mean * mean;
    ln_scalars(mean, var, mb[i], ri[i]);
  }
  f32x4 acc[2][6], acch = {0, 0, 0, 0};
#pragma unroll
  for (int c = 0; c < 6; ++c) acc[0][c] = acc[1][c] = (f32x4){0, 0, 0, 0};
  const char LAS* wbh = ((w & 1) ? wb1 : wb0) + (1 + (w >> 1)) * 32768;
#pragma unroll
  for (int s = 0; s < 16; ++s) {
    v4u n0, n1, nh;
#pragma unroll
    for (int e = 0; e < 4; ++e) {
      unsigned d0 = pack2(bf_lo(xa[0][s][e]) - mb[0], bf_hi(xa[0][s][e]) - mb[0]);
      n0[e] = pack2(bf_lo(d0) * ri[0], bf_hi(d0) * ri[0]);
      unsigned d1 = pack2(bf_lo(xa[1][s][e]) - mb[1], bf_hi(xa[1][s][e]) - mb[1]);
      n1[e] = pack2(bf_lo(d1) * ri[1], bf_hi(d1) * ri[1]);
      unsigned d2 = pack2(bf_lo(xh[s][e]) - mb[2], bf_hi(xh[s][e]) - mb[2]);
      nh[e] = pack2(bf_lo(d2) * ri[2], bf_hi(d2) * ri[2]);
    }
    const v8s a0 = __builtin_bit_cast(v8s, n0), a1 = __builtin_bit_cast(v8s, n1),
              ah = __builtin_bit_cast(v8s, nh);
#pragma unroll
    for (int c = 0; c < 6; ++c) {
      v8s wf = trfrag((c & 1) ? wb1 : wb0, (c >> 1) * 32768 + s * 2048);
      acc[0][c] = __builtin_amdgcn_mfma_f32_16x16x32_bf16(wf, a0, acc[0][c], 0, 0, 0);
      acc[1][c] = __builtin_amdgcn_mfma_f32_16x16x32_bf16(wf, a1, acc[1][c], 0, 0, 0);
    }
    {
      v8s wf = trfrag(wbh, s * 2048);
      acch = __builtin_amdgcn_mfma_f32_16x16x32_bf16(wf, ah, acch, 0, 0, 0);
    }
  }
  __syncthreads();
  char LAS* qt = sm + 98304;
#pragma unroll
  for (int c = 0; c < 6; ++c) {
#pragma unroll
    for (int i = 0; i < 2; ++i) {
      f32x4 v = acc[i][c];
      v2u o = {pack2(v[0], v[1]), pack2(v[2], v[3])};
      *(v2u LAS*)(qt + (16 + 32 * w + 16 * i + (l & 15)) * QROW + c * 32 + 8 * g4) = o;
    }
    if (c == 2 + w) {
      f32x4 v = acch;
      v2u o = {pack2(v[0], v[1]), pack2(v[2], v[3])};
      *(v2u LAS*)(qt + (l & 15) * QROW + c * 32 + 8 * g4) = o;
    }
  }
  __syncthreads();
  {
    const int hh = tid >> 7, i = tid & 127;
    const char LAS* rp = qt + (16 + i) * QROW + hh * 32;
    const v4u q0 = *(const v4u LAS*)(rp), q1 = *(const v4u LAS*)(rp + 16);
    float lg[8];
#pragma unroll
    for (int k = 0; k < 8; ++k) {
      const v4u k0 = *(const v4u LAS*)(rp - 2 * k * QROW + 64), k1 = *(const v4u LAS*)(rp - 2 * k * QROW + 80);
      float d = 0.f;
#pragma unroll
      for (int e = 0; e < 4; ++e) d = dot2(q0[e], k0[e], d);
#pragma unroll
      for (int e = 0; e < 4; ++e) d = dot2(q1[e], k1[e], d);
      d = rbf(d) * 0.25f;
      lg[k] = (t0 + i - 2 * k >= 0) ? d : -1e9f;
    }
    float m = lg[0];
#pragma unroll
    for (int k = 1; k < 8; ++k) m = fmaxf(m, lg[k]);
    float sum = 0.f;
#pragma unroll
    for (int k = 0; k < 8; ++k) { lg[k] = __expf(lg[k] - m); sum += lg[k]; }
    const float inv = 1.0f / sum;
    float o[16];
#pragma unroll
    for (int e = 0; e < 16; ++e) o[e] = 0.f;
#pragma unroll
    for (int k = 0; k < 8; ++k) {
      const float p = rbf(lg[k] * inv);
      const v4u v0 = *(const v4u LAS*)(rp - 2 * k * QROW + 128), v1 = *(const v4u LAS*)(rp - 2 * k * QROW + 144);
#pragma unroll
      for (int e = 0; e < 4; ++e) {
        o[2 * e] += p * bf_lo(v0[e]); o[2 * e + 1] += p * bf_hi(v0[e]);
        o[8 + 2 * e] += p * bf_lo(v1[e]); o[8 + 2 * e + 1] += p * bf_hi(v1[e]);
      }
    }
    v4u r0 = {pack2(o[0], o[1]), pack2(o[2], o[3]), pack2(o[4], o[5]), pack2(o[6], o[7])};
    v4u r1 = {pack2(o[8], o[9]), pack2(o[10], o[11]), pack2(o[12], o[13]), pack2(o[14], o[15])};
    u16* op = O + (size_t)(t0 + i) * D + g * 32 + hh * 16;
    *(v4u*)op = r0;
    *(v4u*)(op + 8) = r1;
  }
}

constexpr int K2_LDS = 65536;
__global__ __launch_bounds__(256, 1) void k_wo_ln(const u16* __restrict__ O, const u16* __restrict__ Wo,
                                                  const u16* __restrict__ X, const u16* __restrict__ Wr,
                                                  u16* __restrict__ X1, u16* __restrict__ X2,
                                                  unsigned* xch, float* __restrict__ rs) {
  extern __shared__ __attribute__((aligned(16))) char smem[];
  char LAS* sm = (char LAS*)smem;
  const int tid = threadIdx.x, l = tid & 63, w = __builtin_amdgcn_readfirstlane(tid >> 6);
  const int b = blockIdx.x, xcd = b & 7, j = b >> 3;
  const int panel = 4 * xcd + (j >> 3), part = j & 7;
  const int r0 = 64 * panel + 16 * w, c0 = 64 * part;
  {
    const int row = l >> 2, c16 = (l & 3) ^ (((row >> 3) & 1) << 1);
    const u16* src = Wo + (size_t)row * D + c0 + c16 * 8;
#pragma unroll
    for (int i = 0; i < 16; ++i) {
      const int item = 4 * i + w, kb = item >> 1, p = item & 1;
      glds16(src + (size_t)kb * 16 * D + p * 32, sm + p * 32768 + kb * 1024);
    }
  }
  const int g4 = l >> 4, q = (l & 15) >> 2, pp = l & 3;
  const int row = r0 + (l & 15);
  v4u xa[16];
  {
    const u16* p0 = O + (size_t)row * D + 8 * g4;
#pragma unroll
    for (int s = 0; s < 16; ++s) xa[s] = *(const v4u*)(p0 + 32 * s);
  }
  v2u xr[4], wr[4];
#pragma unroll
  for (int c = 0; c < 4; ++c) {
    xr[c] = *(const v2u*)(X + (size_t)row * D + c0 + 16 * c + 4 * g4);
    wr[c] = *(const v2u*)(Wr + c0 + 16 * c + 4 * g4);
  }
  const v4u wall = *(const v4u*)(Wr + 8 * l);
  asm volatile("s_waitcnt vmcnt(0)" ::: "memory");
  __syncthreads();
  const char LAS* wb0 = sm + (8 * g4 + q) * 64 + (((pp >> 1) ^ ((g4 & 1) << 1)) * 16) + 8 * (pp & 1);
  const char LAS* wb1 = sm + (8 * g4 + q) * 64 + ((((pp >> 1) ^ ((g4 & 1) << 1)) ^ 2) * 16) + 8 * (pp & 1);
  f32x4 acc[4];
#pragma unroll
  for (int c = 0; c < 4; ++c) acc[c] = (f32x4){0, 0, 0, 0};
#pragma unroll
  for (int s = 0; s < 16; ++s) {
    const v8s a0 = __builtin_bit_cast(v8s, xa[s]);
#pragma unroll
    for (int c = 0; c < 4; ++c) {
      v8s wf = trfrag((c & 1) ? wb1 : wb0, (c >> 1) * 32768 + s * 2048);
      acc[c] = __builtin_amdgcn_mfma_f32_16x16x32_bf16(wf, a0, acc[c], 0, 0, 0);
    }
  }
  float sw = 0.f;
#pragma unroll
  for (int e = 0; e < 4; ++e) sw = dot2(wall[e], 0x3f803f80u, sw);
#pragma unroll
  for (int o = 1; o < 64; o <<= 1) sw += __shfl_xor(sw, o);
  float x1[16];
  float s1 = 0.f, dt = 0.f;
#pragma unroll
  for (int c = 0; c < 4; ++c) {
    x1[4 * c + 0] = rbf(bf_lo(xr[c][0]) + rbf(acc[c][0]));
    x1[4 * c + 1] = rbf(bf_hi(xr[c][0]) + rbf(acc[c][1]));
    x1[4 * c + 2] = rbf(bf_lo(xr[c][1]) + rbf(acc[c][2]));
    x1[4 * c + 3] = rbf(bf_hi(xr[c][1]) + rbf(acc[c][3]));
    dt += x1[4 * c] * bf_lo(wr[c][0]) + x1[4 * c + 1] * bf_hi(wr[c][0]) + x1[4 * c + 2] * bf_lo(wr[c][1]) +
          x1[4 * c + 3] * bf_hi(wr[c][1]);
    s1 += (x1[4 * c] + x1[4 * c + 1]) + (x1[4 * c + 2] + x1[4 * c + 3]);
  }
  s1 += __shfl_xor(s1, 16); dt += __shfl_xor(dt, 16);
  s1 += __shfl_xor(s1, 32); dt += __shfl_xor(dt, 32);
  const float lm = s1 * (1.0f / 64);
  float m2 = 0.f;
#pragma unroll
  for (int e = 0; e < 16; ++e) m2 += (x1[e] - lm) * (x1[e] - lm);
  m2 += __shfl_xor(m2, 16);
  m2 += __shfl_xor(m2, 32);
  const __amdgpu_buffer_rsrc_t xr_rsrc = __builtin_amdgcn_make_buffer_rsrc(xch, 0, S * 8 * 16, 0x00020000);
  if (g4 == 0) {
    unsigned u0 = __float_as_uint(s1), u1 = __float_as_uint(m2), u2 = __float_as_uint(dt);
    u0 = u0 == SENT ? 0x7fc00000u : u0; u1 = u1 == SENT ? 0x7fc00000u : u1; u2 = u2 == SENT ? 0x7fc00000u : u2;
    v4u v = {u0, u1, u2, 0u};
    __builtin_amdgcn_raw_buffer_store_b128(v, xr_rsrc, (row * 8 + part) * 16, 0, 16);
  }
#pragma unroll
  for (int c = 0; c < 4; ++c) {
    v2u o = {pack2(x1[4 * c], x1[4 * c + 1]), pack2(x1[4 * c + 2], x1[4 * c + 3])};
    *(v2u*)(X1 + (size_t)row * D + c0 + 16 * c + 4 * g4) = o;
  }
  v4u e0, e1;
  for (unsigned spins = 0;; ++spins) {
    e0 = __builtin_amdgcn_raw_buffer_load_b128(xr_rsrc, (row * 8 + 2 * g4) * 16, 0, 16);
    e1 = __builtin_amdgcn_raw_buffer_load_b128(xr_rsrc, (row * 8 + 2 * g4 + 1) * 16, 0, 16);
    bool ok = e0[0] != SENT && e0[1] != SENT && e0[2] != SENT && e1[0] != SENT && e1[1] != SENT && e1[2] != SENT;
    if (__all(ok) || spins > (1u << 20)) break;
    __builtin_amdgcn_s_sleep(1);
  }
  float ts = __uint_as_float(e0[0]) + __uint_as_float(e1[0]);
  float td = __uint_as_float(e0[2]) + __uint_as_float(e1[2]);
  ts += __shfl_xor(ts, 16); td += __shfl_xor(td, 16);
  ts += __shfl_xor(ts, 32); td += __shfl_xor(td, 32);
  const float mean = ts * (1.0f / D);
  const float d0 = __uint_as_float(e0[0]) * (1.0f / 64) - mean, d1 = __uint_as_float(e1[0]) * (1.0f / 64) - mean;
  float tm = __uint_as_float(e0[1]) + __uint_as_float(e1[1]) + 64.f * (d0 * d0 + d1 * d1);
  tm += __shfl_xor(tm, 16);
  tm += __shfl_xor(tm, 32);
  float mb, ri;
  ln_scalars(mean, tm * (1.0f / D), mb, ri);
#pragma unroll
  for (int c = 0; c < 4; ++c) {
    float z[4];
#pragma unroll
    for (int e = 0; e < 4; ++e) {
      const float y = rbf(rbf(x1[4 * c + e] - mb) * ri);
      z[e] = x1[4 * c + e] + y;
    }
    v2u o = {pack2(z[0], z[1]), pack2(z[2], z[3])};
    *(v2u*)(X2 + (size_t)row * D + c0 + 16 * c + 4 * g4) = o;
  }
  if (part == 0 && g4 == 0) {
    const float r = rbf((td - mb * sw) * ri);
    rs[row] = r;
    rs[S + row] = mb;
    rs[2 * S + row] = ri;
  }
}

__device__ __forceinline__ unsigned wave_max_u32(unsigned v) {
#pragma unroll
  for (int o = 1; o < 64; o <<= 1) { unsigned t = __shfl_xor(v, o); v = v > t ? v : t; }
  return v;
}
__global__ __launch_bounds__(256, 1) void k_gate_up(const float* __restrict__ rs, const u16* __restrict__ X1,
                                                    const u16* __restrict__ Wg, const u16* __restrict__ Wu,
                                                    u16* __restrict__ Hh, int* __restrict__ sel) {
  __shared__ __attribute__((aligned(16))) char smem[16384 + 4096 + 256];
  char LAS* sm = (char LAS*)smem;
  const int tid = threadIdx.x, l = tid & 63, w = __builtin_amdgcn_readfirstlane(tid >> 6);
  const int b = blockIdx.x;
  {
    const u16* src = ((l & 1) ? Wu : Wg) + (size_t)(l >> 1) * F + 8 * b;
#pragma unroll
    for (int i = 0; i < 4; ++i) {
      const int kb = 4 * w + i;
      glds16(src + (size_t)kb * 32 * F, sm + kb * 1024);
    }
  }
  unsigned key[8];
  {
    const f32x4 a = *(const f32x4*)(rs + 8 * tid), c = *(const f32x4*)(rs + 8 * tid + 4);
#pragma unroll
    for (int e = 0; e < 8; ++e) {
      unsigned u = __float_as_uint(e < 4 ? a[e] : c[e - 4]) >> 16;
      u = (u & 0x8000u) ? (u ^ 0xffffu) : (u ^ 0x8000u);
      key[e] = (u << 16) | (unsigned)(0xffff - (8 * tid + e));
    }
  }
  unsigned LAS* cand = (unsigned LAS*)(sm + 16384 + 4096);
  for (int it = 0; it < 8; ++it) {
    unsigned m = key[0];
#pragma unroll
    for (int e = 1; e < 8; ++e) m = m > key[e] ? m : key[e];
    m = wave_max_u32(m);
#pragma unroll
    for (int e = 0; e < 8; ++e) key[e] = key[e] == m ? 0u : key[e];
    if (l == 0) cand[w * 8 + it] = m;
  }
  __syncthreads();
  unsigned ck = l < 32 ? cand[l] : 0u;
  int mytok = 0;
  for (int it = 0; it < 8; ++it) {
    unsigned m = wave_max_u32(ck);
    ck = ck == m ? 0u : ck;
    if ((l & 7) == it) mytok = 0xffff - (int)(m & 0xffffu);
  }
  if (b == 0 && tid < 8) sel[tid] = mytok;
  const int g4 = l >> 4, q = (l & 15) >> 2, pp = l & 3;
  const float mb = rs[S + mytok], ri = rs[2 * S + mytok];
  v4u xa[4];
#pragma unroll
  for (int s = 0; s < 4; ++s) xa[s] = *(const v4u*)(X1 + (size_t)mytok * D + 128 * w + 32 * s + 8 * g4);
  asm volatile("s_waitcnt vmcnt(0)" ::: "memory");
  f32x4 acc = {0, 0, 0, 0};
  const char LAS* wb = sm + w * 4096 + (8 * g4 + q) * 32 + 8 * pp;
#pragma unroll
  for (int s = 0; s < 4; ++s) {
    v4u y;
#pragma unroll
    for (int e = 0; e < 4; ++e)
      y[e] = pack2(rbf(bf_lo(xa[s][e]) - mb) * ri, rbf(bf_hi(xa[s][e]) - mb) * ri);
    v4s a = __builtin_amdgcn_ds_read_tr16_b64_v4i16((LAS v4s*)(wb + s * 1024));
    v4s c = __builtin_amdgcn_ds_read_tr16_b64_v4i16((LAS v4s*)(wb + s * 1024 + 128));
    v8s wf = {a[0], a[1], a[2], a[3], c[0], c[1], c[2], c[3]};
    acc = __builtin_amdgcn_mfma_f32_16x16x32_bf16(wf, __builtin_bit_cast(v8s, y), acc, 0, 0, 0);
  }
  f32x4 LAS* red = (f32x4 LAS*)(sm + 16384);
  red[w * 64 + l] = acc;
  __syncthreads();
  if (tid < 32 && (tid & 15) < 8) {
    f32x4 gt = red[tid] + red[64 + tid] + red[128 + tid] + red[192 + tid];
    f32x4 up = red[tid + 32] + red[64 + tid + 32] + red[128 + tid + 32] + red[192 + tid + 32];
    float h[4];
#pragma unroll
    for (int e = 0; e < 4; ++e) {
      const float gv = rbf(gt[e]);
      const float sg = rbf(gv / (1.0f + __expf(-gv)));
      h[e] = sg * rbf(up[e]);
    }
    v2u o = {pack2(h[0], h[1]), pack2(h[2], h[3])};
    *(v2u*)(Hh + (size_t)(tid & 15) * F + 8 * b + 4 * (tid >> 4)) = o;
  }
}

__global__ __launch_bounds__(256, 1) void k_down(const float* __restrict__ rs, const u16* __restrict__ X1,
                                                 const u16* __restrict__ Wd, const u16* __restrict__ Hh,
                                                 const int* __restrict__ sel, u16* __restrict__ X2) {
  __shared__ __attribute__((aligned(16))) char smem[57344 + 4096];
  char LAS* sm = (char LAS*)smem;
  const int tid = threadIdx.x, l = tid & 63, w = __builtin_amdgcn_readfirstlane(tid >> 6);
  const int b = blockIdx.x;
  {
    const u16* src = Wd + (size_t)(l >> 1) * D + 16 * b + 8 * (l & 1);
#pragma unroll
    for (int i = 0; i < 14; ++i) {
      const int kb = 14 * w + i;
      glds16(src + (size_t)kb * 32 * D, sm + kb * 1024);
    }
  }
  const int g4 = l >> 4, q = (l & 15) >> 2, pp = l & 3;
  v4u ha[14];
#pragma unroll
  for (int s = 0; s < 14; ++s) ha[s] = *(const v4u*)(Hh + (size_t)(l & 7) * F + 448 * w + 32 * s + 8 * g4);
  const int tok = sel[l & 7];
  const float r = rs[tok], mb = rs[S + tok], ri = rs[2 * S + tok];
  const v2u xv = *(const v2u*)(X1 + (size_t)tok * D + 16 * b + 4 * g4);
  asm volatile("s_waitcnt vmcnt(0)" ::: "memory");
  f32x4 acc = {0, 0, 0, 0};
  const char LAS* wb = sm + w * 14336 + (8 * g4 + q) * 32 + 8 * pp;
#pragma unroll
  for (int s = 0; s < 14; ++s) {
    v4s a = __builtin_amdgcn_ds_read_tr16_b64_v4i16((LAS v4s*)(wb + s * 1024));
    v4s c = __builtin_amdgcn_ds_read_tr16_b64_v4i16((LAS v4s*)(wb + s * 1024 + 128));
    v8s wf = {a[0], a[1], a[2], a[3], c[0], c[1], c[2], c[3]};
    acc = __builtin_amdgcn_mfma_f32_16x16x32_bf16(wf, __builtin_bit_cast(v8s, ha[s]), acc, 0, 0, 0);
  }
  f32x4 LAS* red = (f32x4 LAS*)(sm + 57344);
  red[w * 64 + l] = acc;
  __syncthreads();
  if (w == 0 && (l & 15) < 8) {
    f32x4 y = red[l] + red[64 + l] + red[128 + l] + red[192 + l];
    const float gt = rbf(1.0f / (1.0f + __expf(-r)));
    float x1[4] = {bf_lo(xv[0]), bf_hi(xv[0]), bf_lo(xv[1]), bf_hi(xv[1])};
    float z[4];
#pragma unroll
    for (int e = 0; e < 4; ++e) {
      const float y1 = rbf(rbf(x1[e] - mb) * ri);
      const float up = rbf(rbf(y[e]) * gt);
      z[e] = x1[e] + rbf(y1 + up);
    }
    v2u o = {pack2(z[0], z[1]), pack2(z[2], z[3])};
    *(v2u*)(X2 + (size_t)tok * D + 16 * b + 4 * g4) = o;
  }
}

extern "C" void kernel_launch(void* const* d_in, const int* in_sizes, int n_in, void* d_out, int out_size,
                              void* d_ws, size_t ws_size, hipStream_t stream) {
  const u16* x = (const u16*)d_in[0];
  const u16* wqkv = (const u16*)d_in[1];
  const u16* wo = (const u16*)d_in[2];
  const u16* wr = (const u16*)d_in[4];
  const u16* wg = (const u16*)d_in[5];
  const u16* wu = (const u16*)d_in[6];
  const u16* wd = (const u16*)d_in[7];
  char* ws = (char*)d_ws;
  u16* O = (u16*)(ws);
  u16* X1 = (u16*)(ws + (2 << 20));
  u16* X2 = (u16*)(ws + (4 << 20));
  unsigned* xch = (unsigned*)(ws + (6 << 20));
  float* rs = (float*)(ws + (7 << 20));
  u16* Hh = (u16*)(ws + (7 << 20) + 65536);
  int* sel = (int*)(ws + (7 << 20) + 131072);
  u16* out = (u16*)d_out;
  (void)hipFuncSetAttribute((const void*)k_qkv_attn, hipFuncAttributeMaxDynamicSharedMemorySize, K1_LDS);
  (void)hipFuncSetAttribute((const void*)k_wo_ln, hipFuncAttributeMaxDynamicSharedMemorySize, K2_LDS);
  for (int rnd = 0; rnd < 2; ++rnd) {
    const u16* xin = rnd ? X2 : x;
    u16* xout = rnd ? out : X2;
    k_qkv_attn<<<256, 256, K1_LDS, stream>>>(xin, wqkv, O, xch);
    k_wo_ln<<<256, 256, K2_LDS, stream>>>(O, wo, xin, wr, X1, xout, xch, rs);
    k_gate_up<<<224, 256, 0, stream>>>(rs, X1, wg, wu, Hh, sel);
    k_down<<<32, 256, 0, stream>>>(rs, X1, wd, Hh, sel, xout);
  }
}
```

```cpp
#include <hip/hip_runtime.h>
#include <hip/hip_bf16.h>
#include <stdint.h>

#define LAS __attribute__((address_space(3)))
typedef unsigned short u16;
typedef short v4s __attribute__((ext_vector_type(4)));
typedef short v8s __attribute__((ext_vector_type(8)));
typedef __bf16 v2bf __attribute__((ext_vector_type(2)));
typedef float f32x4 __attribute__((ext_vector_type(4)));
typedef float f32x2 __attribute__((ext_vector_type(2)));
typedef unsigned v4u __attribute__((ext_vector_type(4)));
typedef unsigned v2u __attribute__((ext_vector_type(2)));

#define STAMP(k, i)
#define TS(v)
#define TSPUT(k, i, v)

constexpr int S = 2048, D = 512, F = 1792;
constexpr unsigned SENT = 0xFFFFFFFFu;
constexpr unsigned SPIN = 1u << 18;
constexpr unsigned OFF_Z1 = 0, OFF_Z2 = 2u << 20, OFF_OF = 4u << 20, OFF_AA = 8u << 20, OFF_X2 = 12u << 20,
                   OFF_SM = 14u << 20, WS_BYTES = 15u << 20;
constexpr unsigned SM_R = OFF_SM, SM_ST = OFF_SM + 16384, SM_HF = OFF_SM + 49152, SM_X2S = OFF_SM + 114688,
                   SM_IDX = OFF_SM + 122880, SM_FZ = OFF_SM + 126976;
constexpr int L_W = 0, L_SH = 98304, L_F = 131072, L_TOTAL = 163840;
constexpr int PLANE = 2304;

__device__ __forceinline__ float bf_lo(unsigned u) { return __uint_as_float(u << 16); }
__device__ __forceinline__ float bf_hi(unsigned u) { return __uint_as_float(u & 0xffff0000u); }
__device__ __forceinline__ unsigned pack2(float a, float b) {
  f32x2 f = {a, b};
  v2bf c = __builtin_convertvector(f, v2bf);
  return __builtin_bit_cast(unsigned, c);
}
__device__ __forceinline__ float rbf(float a) {
  __bf16 c = (__bf16)a;
  return __uint_as_float(((unsigned)__builtin_bit_cast(u16, c)) << 16);
}
__device__ __forceinline__ float dot2(unsigned a, unsigned b, float c) {
  return __builtin_amdgcn_fdot2_f32_bf16(__builtin_bit_cast(v2bf, a), __builtin_bit_cast(v2bf, b), c, false);
}
__device__ __forceinline__ v8s tr2(const char LAS* p, int second) {
  v4s a = __builtin_amdgcn_ds_read_tr16_b64_v4i16((LAS v4s*)p);
  v4s b = __builtin_amdgcn_ds_read_tr16_b64_v4i16((LAS v4s*)(p + second));
  v8s r = {a[0], a[1], a[2], a[3], b[0], b[1], b[2], b[3]};
  return r;
}
__device__ __forceinline__ f32x4 mfma(v8s a, v4u b, f32x4 c) {
  return __builtin_amdgcn_mfma_f32_16x16x32_bf16(a, __builtin_bit_cast(v8s, b), c, 0, 0, 0);
}
typedef __amdgpu_buffer_rsrc_t srd_t;
__device__ __forceinline__ srd_t make_srd(const void* p, unsigned bytes) {
  return __builtin_amdgcn_make_buffer_rsrc((void*)p, 0, bytes, 0x00020000);
}
__device__ __forceinline__ void dma16(srd_t srd, unsigned voff, unsigned soff, unsigned lds) {
  unsigned keep;
  asm volatile("s_mov_b32 %0, m0\n\ts_mov_b32 m0, %3\n\ts_nop 0\n\tbuffer_load_dwordx4 %1, %2, %4 offen lds\n\ts_mov_b32 m0, %0"
               : "=&s"(keep) : "v"(voff), "s"(srd), "s"(lds), "s"(soff) : "memory");
}
__device__ __forceinline__ void aload16(v4u& d, srd_t srd, unsigned voff, unsigned soff) {
  asm volatile("buffer_load_dwordx4 %0, %1, %2, %3 offen" : "=v"(d) : "v"(voff), "s"(srd), "s"(soff) : "memory");
}
__device__ __forceinline__ void aload16_sc1(v4u& d, srd_t srd, unsigned voff, unsigned soff) {
  asm volatile("buffer_load_dwordx4 %0, %1, %2, %3 offen sc1" : "=v"(d) : "v"(voff), "s"(srd), "s"(soff) : "memory");
}
#define SLEEP(n) do { __builtin_amdgcn_s_sleep(n); asm volatile("" ::: "memory"); } while (0)
#define VMWAIT(n) asm volatile("s_waitcnt vmcnt(" #n ")" ::: "memory")
#define BAR() do { asm volatile("s_waitcnt lgkmcnt(0)" ::: "memory"); __builtin_amdgcn_s_barrier(); asm volatile("" ::: "memory"); } while (0)
__device__ __forceinline__ bool valid4(v4u v) { return v[0] != SENT && v[1] != SENT && v[2] != SENT && v[3] != SENT; }

template <int CTRL, int RMASK>
__device__ __forceinline__ float dpp_add(float v) {
  int t = __builtin_amdgcn_update_dpp(0, __float_as_int(v), CTRL, RMASK, 0xf, true);
  return v + __int_as_float(t);
}
__device__ __forceinline__ float wave_sum(float v) {
  v = dpp_add<0x111, 0xf>(v); v = dpp_add<0x112, 0xf>(v); v = dpp_add<0x114, 0xf>(v); v = dpp_add<0x118, 0xf>(v);
  v = dpp_add<0x142, 0xa>(v); v = dpp_add<0x143, 0xc>(v);
  return __int_as_float(__builtin_amdgcn_readlane(__float_as_int(v), 63));
}
template <int CTRL, int RMASK>
__device__ __forceinline__ unsigned dpp_max(unsigned v) {
  unsigned t = (unsigned)__builtin_amdgcn_update_dpp(0, (int)v, CTRL, RMASK, 0xf, true);
  return v > t ? v : t;
}
__device__ __forceinline__ unsigned wave_max(unsigned v) {
  v = dpp_max<0x111, 0xf>(v); v = dpp_max<0x112, 0xf>(v); v = dpp_max<0x114, 0xf>(v); v = dpp_max<0x118, 0xf>(v);
  v = dpp_max<0x142, 0xa>(v); v = dpp_max<0x143, 0xc>(v);
  return (unsigned)__builtin_amdgcn_readlane((int)v, 63);
}
__device__ __forceinline__ void ln_row(const float (&x)[8], float (&y)[8], float& mb, float& ri) {
  float s = ((x[0] + x[1]) + (x[2] + x[3])) + ((x[4] + x[5]) + (x[6] + x[7]));
  const float mean = wave_sum(s) * (1.0f / D);
  float q = 0.f;
#pragma unroll
  for (int e = 0; e < 8; ++e) q += (x[e] - mean) * (x[e] - mean);
  const float var = wave_sum(q) * (1.0f / D);
  mb = rbf(mean);
  const float sg = rbf(sqrtf(rbf(rbf(var) + 1e-5f)));
  ri = 1.0f / sg;
#pragma unroll
  for (int e = 0; e < 8; ++e) y[e] = rbf(rbf(x[e] - mb) * ri);
}
__device__ __forceinline__ void unpack8(v4u v, float (&x)[8]) {
#pragma unroll
  for (int e = 0; e < 4; ++e) { x[2 * e] = bf_lo(v[e]); x[2 * e + 1] = bf_hi(v[e]); }
}
__device__ __forceinline__ v4u pack8(const float (&x)[8]) {
  v4u v = {pack2(x[0], x[1]), pack2(x[2], x[3]), pack2(x[4], x[5]), pack2(x[6], x[7])};
  return v;
}

__global__ __launch_bounds__(512, 2) void k_prep(const u16* __restrict__ X, const u16* __restrict__ Wqkv,
                                                 const u16* __restrict__ Wo, const u16* __restrict__ Wg,
                                                 const u16* __restrict__ Wu, const u16* __restrict__ Wd,
                                                 char* __restrict__ ws) {
  __shared__ __attribute__((aligned(16))) char sm[8 * 1040];
  const int tid = threadIdx.x, l = tid & 63, w = tid >> 6;
  const int b = blockIdx.x, xcd = b & 7, jj = b >> 3;
  const int t = 4 * (xcd >> 1) + (jj >> 3), j = 8 * (xcd & 1) + (jj & 7);
  STAMP(0, 0);
  const v4u xv = *(const v4u*)(X + (size_t)(128 * t + 8 * j + w) * D + 8 * l);
  unsigned dummy;
  {
    const u16* p;
    if (tid < 192) {
      const int i = jj * 192 + tid, k = i / 12, m = i % 12;
      p = Wqkv + (size_t)k * 1536 + (m >> 2) * 512 + (xcd & 1) * 256 + (m & 3) * 64;
    } else if (tid < 320) {
      p = Wo + (size_t)(jj * 128 + tid - 192) * 64;
    } else if (tid < 448) {
      const int i = jj * 128 + tid - 320, k = i & 511, m = i >> 9;
      int grp = xcd + 8 * (m >> 1);
      grp = grp < 28 ? grp : xcd;
      p = ((m & 1) ? Wu : Wg) + (size_t)k * F + grp * 64;
    } else {
      int k = jj * 56 + tid - 448;
      k = k < F ? k : F - 1;
      p = Wd + (size_t)k * D + xcd * 64;
    }
    asm volatile("global_load_dword %0, %1, off" : "=v"(dummy) : "v"(p) : "memory");
  }
  const v4u sv = {SENT, SENT, SENT, SENT};
  *(v4u*)(ws + OFF_OF + (size_t)b * 8192 + tid * 16) = sv;
  if (tid < 36) *(v4u*)(ws + OFF_SM + b * 576 + tid * 16) = sv;
  float x[8], y[8], mb, ri;
  unpack8(xv, x);
  ln_row(x, y, mb, ri);
  *(v4u*)(sm + w * 1040 + l * 16) = pack8(y);
  __syncthreads();
  {
    const int chunk = 8 * w + (l >> 3), r8 = l & 7;
    const v4u v = *(const v4u*)(sm + r8 * 1040 + chunk * 16);
    const int rt = 8 * t + (j >> 1);
    *(v4u*)(ws + OFF_Z1 + (size_t)(((rt * 16 + (chunk >> 2)) * 64 + (chunk & 3) * 16 + 8 * (j & 1) + r8) * 16)) = v;
  }
  asm volatile("s_waitcnt vmcnt(0)" : "+v"(dummy) :: "memory");
  STAMP(0, 1);
}

__global__ __launch_bounds__(512, 2) void k_main(const u16* __restrict__ X, const u16* __restrict__ Wqkv,
                                                 const u16* __restrict__ Wo, const u16* __restrict__ Wr,
                                                 const u16* __restrict__ Wg, const u16* __restrict__ Wu,
                                                 const u16* __restrict__ Wd, u16* __restrict__ Out,
                                                 char* __restrict__ ws) {
  extern __shared__ __attribute__((aligned(16))) char smem[];
  char LAS* sm = (char LAS*)smem;
  const int w0 = __builtin_amdgcn_readfirstlane(threadIdx.x >> 6);
  const srd_t rws = make_srd(ws, WS_BYTES);
#define IDS                                                                                              \
  int tid = threadIdx.x, w = w0, b = blockIdx.x;                                                         \
  asm volatile("" : "+v"(tid)); asm volatile("" : "+s"(w)); asm volatile("" : "+s"(b));                  \
  const int l = tid & 63, xcd = b & 7, jj = b >> 3;                                                      \
  const int t = 4 * (xcd >> 1) + (jj >> 3), j = 8 * (xcd & 1) + (jj & 7);                                \
  const int g4 = l >> 4, tok = l & 15, qd = (l & 15) >> 2, pp = l & 3;                                   \
  const int rq = j >> 2, cq = j & 3, myrow = 128 * t + 8 * j + w;                                        \
  const int gu_grp = xcd + 8 * (jj >> 2);                                                                \
  const bool is_gu = jj < 16 && gu_grp < 28, is_dn = jj >= 16 && jj < 24;                                \
  const int gu_u = 4 * gu_grp + (jj & 3), dn_u = 8 * xcd + (jj - 16);                                    \
  const unsigned lane16 = l * 16;                                                                        \
  const unsigned wvoff = (unsigned)((l >> 2) * 3072 + (((l & 3) ^ (((l >> 5) & 1) << 1)) * 16));         \
  const char LAS* wb0 = sm + L_W + (8 * g4 + qd) * 64 + (((pp >> 1) ^ ((g4 & 1) << 1)) * 16) + 8 * (pp & 1);       \
  const char LAS* wb1 = sm + L_W + (8 * g4 + qd) * 64 + ((((pp >> 1) ^ ((g4 & 1) << 1)) ^ 2) * 16) + 8 * (pp & 1); \
  const char LAS* wbh = ((w & 1) ? wb1 : wb0) + (1 + ((w >> 1) & 1)) * 32768;                            \
  const int rth = t > 0 ? 8 * t - 1 : 0;                                                                 \
  const unsigned wovoff = (unsigned)(g4 * 1024 + ((tok ^ ((g4 | (((w >> 1) & 1) << 2)) << 1)) * 16));    \
  (void)rq; (void)cq; (void)myrow; (void)is_gu; (void)is_dn; (void)gu_u; (void)dn_u; (void)lane16; (void)wvoff; \
  (void)wb0; (void)wb1; (void)wbh; (void)rth; (void)wovoff; (void)qd; (void)pp; (void)tok; (void)g4;
  STAMP(1, 0);

  v2u pq[6], ph;
  v4u xres, wrv;
  v8s wof[16];
  {
    IDS
    const srd_t srd_w = make_srd(Wqkv, 512 * 1536 * 2);
    v4u z[16], zh[2];
    aload16(zh[0], rws, lane16, OFF_Z1 + (rth * 16 + 2 * w) * 1024);
    aload16(zh[1], rws, lane16, OFF_Z1 + (rth * 16 + 2 * w + 1) * 1024);
#pragma unroll
    for (int c = 0; c < 4; ++c) {
#pragma unroll
      for (int p = 0; p < 3; ++p)
        dma16(srd_w, wvoff, (unsigned)((8 * c + w) * 16 * 3072 + p * 1024 + j * 64), L_W + p * 32768 + (8 * c + w) * 1024);
#pragma unroll
      for (int s = 0; s < 4; ++s) aload16(z[4 * c + s], rws, lane16, OFF_Z1 + ((8 * t + w) * 16 + 4 * c + s) * 1024);
    }
    aload16(xres, make_srd(X, S * D * 2), lane16, myrow * 1024);
    aload16(wrv, make_srd(Wr, D * 2), lane16, 0);
    {
      const v4u sv = {SENT, SENT, SENT, SENT};
      const unsigned ao = (unsigned)((128 * t + 32 * rq + 4 * w + g4) * 1024 + cq * 256 + tok * 16);
      asm volatile("buffer_store_dwordx4 %0, %1, %2, %3 offen sc1" :: "v"(sv), "v"(ao), "s"(rws), "s"(OFF_AA) : "memory");
      asm volatile("buffer_store_dwordx4 %0, %1, %2, %3 offen sc1" :: "v"(sv), "v"(ao), "s"(rws), "s"(OFF_AA + (2u << 20)) : "memory");
      asm volatile("buffer_store_dwordx4 %0, %1, %2, %3 offen sc1\n\ts_nop 1" :: "v"(sv), "v"(lane16), "s"(rws),
                   "s"(OFF_OF + (2u << 20) + ((8 * t + w) * 16 + j) * 1024) : "memory");
    }
    {
      const srd_t sa = make_srd(is_dn ? Wd : Wg, (is_gu || is_dn) ? 512 * F * 2 : 0);
      const srd_t sb = make_srd(is_dn ? Wd : Wu, (is_gu || is_dn) ? 512 * F * 2 : 0);
      const unsigned fv = is_dn ? (unsigned)(l * 1024) : (unsigned)((l >> 1) * (F * 2) + (l & 1) * 16);
      const unsigned fs = is_dn ? (unsigned)(224 * w * 1024 + dn_u * 16) : (unsigned)(64 * w * (F * 2) + gu_u * 32);
      const unsigned st = is_dn ? 64 * 1024 : 32 * F * 2;
      const unsigned lb = is_dn ? L_F + 224 * 16 * w : L_F + 2048 * w;
      dma16(sa, fv, fs, lb);
      dma16(sa, fv, fs + st, lb + 1024);
      dma16(sb, fv, is_dn ? fs + 2 * st : fs, is_dn ? lb + 2048 : lb + 16384);
      dma16(sb, fv, is_dn ? fs + 3 * st : fs + st, is_dn ? lb + 3072 : lb + 16384 + 1024);
    }
    unsigned long long tq[10];
    TS(tq[0]);
    f32x4 acc[6], acch = {0, 0, 0, 0};
#pragma unroll
    for (int c = 0; c < 6; ++c) acc[c] = (f32x4){0, 0, 0, 0};
#pragma unroll
    for (int c = 0; c < 4; ++c) {
      if (c == 0) {
        asm volatile("s_waitcnt vmcnt(30)" : "+v"(z[0]), "+v"(z[1]), "+v"(z[2]), "+v"(z[3]), "+v"(zh[0]), "+v"(zh[1]) :: "memory");
        *(v4u LAS*)(sm + L_SH + (2 * w) * 1024 + lane16) = zh[0];
        *(v4u LAS*)(sm + L_SH + (2 * w + 1) * 1024 + lane16) = zh[1];
      } else if (c == 1) asm volatile("s_waitcnt vmcnt(23)" : "+v"(z[4]), "+v"(z[5]), "+v"(z[6]), "+v"(z[7]) :: "memory");
      else if (c == 2) asm volatile("s_waitcnt vmcnt(16)" : "+v"(z[8]), "+v"(z[9]), "+v"(z[10]), "+v"(z[11]) :: "memory");
      else asm volatile("s_waitcnt vmcnt(9)" : "+v"(z[12]), "+v"(z[13]), "+v"(z[14]), "+v"(z[15]) :: "memory");
      TS(tq[1 + 2 * c]);
      BAR();
      TS(tq[2 + 2 * c]);
#pragma unroll
      for (int s4 = 0; s4 < 4; ++s4) {
        const int s = 4 * c + s4;
#pragma unroll
        for (int ct = 0; ct < 6; ++ct)
          acc[ct] = mfma(tr2(((ct & 1) ? wb1 : wb0) + (ct >> 1) * 32768 + s * 2048, 256), z[s], acc[ct]);
      }
      if (w < 4) {
#pragma unroll
        for (int s4 = 0; s4 < 4; ++s4) {
          const int s = 4 * c + s4;
          acch = mfma(tr2(wbh + s * 2048, 256), *(const v4u LAS*)(sm + L_SH + s * 1024 + lane16), acch);
        }
      }
    }
#pragma unroll
    for (int c = 0; c < 6; ++c) pq[c] = (v2u){pack2(acc[c][0], acc[c][1]), pack2(acc[c][2], acc[c][3])};
    ph = (v2u){pack2(acch[0], acch[1]), pack2(acch[2], acch[3])};
    TS(tq[9]);
    asm volatile("s_waitcnt vmcnt(7)" : "+v"(xres), "+v"(wrv) :: "memory");
#pragma unroll
    for (int i = 0; i < 10; ++i) TSPUT(3, i, tq[i]);
  }
  STAMP(1, 1);
  int role;
  { IDS role = is_gu ? 1 : is_dn ? 2 : 0; }
  bool skip_main = false, skip_halo = false, patch = false;
  int patch_row = 0;
  v2u pf = {0u, 0u};

#pragma unroll 1
  for (int r = 0; r < 2; ++r) {
    BAR();
    if (r == 0) {
      IDS
      const srd_t srd_wo = make_srd(Wo, D * D * 2);
#pragma unroll
      for (int q = 0; q < 3; ++q)
#pragma unroll
        for (int m = 0; m < 4; ++m)
          dma16(srd_wo, wovoff, (unsigned)((128 * q + 4 * (w + 8 * m)) * 1024 + cq * 256), L_W + q * 32768 + (w + 8 * m) * 1024);
    }
    {
      IDS
      char LAS* qp = sm + L_SH + (g4 >> 1) * PLANE + 8 * (g4 & 1);
      if (!skip_main) {
#pragma unroll
        for (int c = 0; c < 6; ++c) *(v2u LAS*)(qp + 2 * c * PLANE + (16 + 16 * w + tok) * 16) = pq[c];
      }
      if (w < 4 && !skip_halo) *(v2u LAS*)(qp + 2 * (2 + w) * PLANE + tok * 16) = ph;
      if (patch) *(v2u LAS*)(qp + 2 * w * PLANE + patch_row * 16) = pf;
    }
    BAR();
    STAMP(1 + r, 2);
    {
      IDS
      const char LAS* rp = sm + L_SH + g4 * PLANE + (16 + 16 * w + tok) * 16;
      const v4u q0 = *(const v4u LAS*)rp;
      float lg[8];
#pragma unroll
      for (int k = 0; k < 8; ++k) {
        const v4u k0 = *(const v4u LAS*)(rp + 4 * PLANE - 32 * k);
        float d = 0.f;
#pragma unroll
        for (int e = 0; e < 4; ++e) d = dot2(q0[e], k0[e], d);
        const auto sw = __builtin_amdgcn_permlane16_swap(__float_as_uint(d), __float_as_uint(d), false, false);
        d = __uint_as_float(sw[0]) + __uint_as_float(sw[1]);
        d = rbf(d) * 0.25f;
        lg[k] = (128 * t + 16 * w + tok - 2 * k >= 0) ? d : -1e9f;
      }
      float m = lg[0];
#pragma unroll
      for (int k = 1; k < 8; ++k) m = fmaxf(m, lg[k]);
      float sum = 0.f;
#pragma unroll
      for (int k = 0; k < 8; ++k) { lg[k] = __expf(lg[k] - m); sum += lg[k]; }
      const float inv = 1.0f / sum;
      float o[8];
#pragma unroll
      for (int e = 0; e < 8; ++e) o[e] = 0.f;
#pragma unroll
      for (int k = 0; k < 8; ++k) {
        const float p = rbf(lg[k] * inv);
        const v4u v0 = *(const v4u LAS*)(rp + 8 * PLANE - 32 * k);
#pragma unroll
        for (int e = 0; e < 4; ++e) { o[2 * e] += p * bf_lo(v0[e]); o[2 * e + 1] += p * bf_hi(v0[e]); }
      }
      VMWAIT(0);
      __builtin_amdgcn_raw_buffer_store_b128(pack8(o), rws, lane16, OFF_OF + r * (2u << 20) + ((8 * t + w) * 16 + j) * 1024, 16);
    }
    STAMP(1 + r, 3);
    {
    IDS
    const srd_t srd_wo = make_srd(Wo, D * D * 2);
    v4u of[4];
    const unsigned ofb = OFF_OF + r * (2u << 20) + ((8 * t + 2 * rq) * 16 + 4 * w) * 1024;
    if (r == 0) {
      BAR();
#pragma unroll
      for (int m = 0; m < 4; ++m)
        dma16(srd_wo, wovoff, (unsigned)((384 + 4 * (w + 8 * m)) * 1024 + cq * 256), L_SH + (w + 8 * m) * 1024);
      const char LAS* wp = sm + (8 * g4 + qd) * 256 + ((w ^ (qd | ((g4 & 1) << 2))) * 32) + 8 * pp;
#pragma unroll
      for (int s = 0; s < 12; ++s) wof[s] = tr2(wp + L_W + s * 8192, 1024);
#pragma unroll
      for (int i = 0; i < 4; ++i) aload16_sc1(of[i], rws, lane16, ofb + i * 1024);
      VMWAIT(4);
      BAR();
#pragma unroll
      for (int s = 12; s < 16; ++s) wof[s] = tr2(wp + L_SH + (s - 12) * 8192, 1024);
    } else {
      __builtin_amdgcn_s_sleep(10);
#pragma unroll
      for (int i = 0; i < 4; ++i) aload16_sc1(of[i], rws, lane16, ofb + i * 1024);
    }
    asm volatile("s_waitcnt vmcnt(0)" : "+v"(of[0]), "+v"(of[1]), "+v"(of[2]), "+v"(of[3]) :: "memory");
    for (unsigned spins = 0; spins < SPIN; ++spins) {
      if (__all(valid4(of[0]) && valid4(of[1]) && valid4(of[2]) && valid4(of[3]))) break;
      SLEEP(2);
#pragma unroll
      for (int i = 0; i < 4; ++i) of[i] = __builtin_amdgcn_raw_buffer_load_b128(rws, lane16, ofb + i * 1024, 16);
    }
#pragma unroll
    for (int i = 0; i < 4; ++i) *(v4u LAS*)(sm + L_W + (4 * w + i) * 1024 + lane16) = of[i];
    }
    BAR();
    STAMP(1 + r, 4);
    {
      IDS
      f32x4 a0 = {0, 0, 0, 0}, a1 = {0, 0, 0, 0};
#pragma unroll
      for (int s = 0; s < 16; ++s) {
        a0 = mfma(wof[s], *(const v4u LAS*)(sm + L_W + s * 1024 + lane16), a0);
        a1 = mfma(wof[s], *(const v4u LAS*)(sm + L_W + (16 + s) * 1024 + lane16), a1);
      }
      char LAS* tp = sm + L_SH + tok * 272 + 32 * w + 8 * g4;
      *(v2u LAS*)tp = (v2u){pack2(a0[0], a0[1]), pack2(a0[2], a0[3])};
      *(v2u LAS*)(tp + 16 * 272) = (v2u){pack2(a1[0], a1[1]), pack2(a1[2], a1[3])};
      BAR();
      const v4u v = *(const v4u LAS*)(sm + L_SH + (4 * w + g4) * 272 + tok * 16);
      __builtin_amdgcn_raw_buffer_store_b128(v, rws, (128 * t + 32 * rq + 4 * w + g4) * 1024 + cq * 256 + tok * 16,
                                             OFF_AA + r * (2u << 20), 16);
    }
    STAMP(1 + r, 5);
    v4u xo;
    {
      IDS
      __builtin_amdgcn_s_sleep(10);
      v4u av;
      for (unsigned spins = 0; spins < SPIN; ++spins) {
        av = __builtin_amdgcn_raw_buffer_load_b128(rws, lane16, OFF_AA + r * (2u << 20) + myrow * 1024, 16);
        if (__all(valid4(av))) break;
        SLEEP(2);
      }
      STAMP(1 + r, 6);
      float x1[8], a[8], y[8], mb, ri;
      unpack8(xres, x1);
      unpack8(av, a);
#pragma unroll
      for (int e = 0; e < 8; ++e) x1[e] = rbf(x1[e] + a[e]);
      ln_row(x1, y, mb, ri);
      float wr[8];
      unpack8(wrv, wr);
      float d = 0.f;
#pragma unroll
      for (int e = 0; e < 8; ++e) d += y[e] * wr[e];
      d = wave_sum(d);
      unsigned u = __float_as_uint(rbf(d)) >> 16;
      u = (u & 0x8000u) ? (u ^ 0xffffu) : (u ^ 0x8000u);
      unsigned key = (u << 16) | (unsigned)(0xfffe - myrow);
      if (l == 0) {
        __builtin_amdgcn_raw_buffer_store_b32(key, rws, myrow * 4, SM_R + r * 8192, 16);
        v2u st = {__float_as_uint(mb), __float_as_uint(ri)};
        st[0] = st[0] == SENT ? 0x7fc00000u : st[0];
        st[1] = st[1] == SENT ? 0x7fc00000u : st[1];
        __builtin_amdgcn_raw_buffer_store_b64(st, rws, myrow * 8, SM_ST + r * 16384, 16);
      }
      STAMP(1 + r, 7);
#pragma unroll
      for (int e = 0; e < 8; ++e) x1[e] = rbf(x1[e] + y[e]);
      xo = pack8(x1);
      if (r == 0) {
        xres = xo;
        __builtin_amdgcn_raw_buffer_store_b128(xo, rws, lane16, OFF_X2 + myrow * 1024, 16);
        ln_row(x1, y, mb, ri);
        *(v4u LAS*)(sm + L_SH + 9216 + w * 1040 + lane16) = pack8(y);
        BAR();
        const int chunk = 8 * w + (l >> 3), r8 = l & 7;
        const v4u v = *(const v4u LAS*)(sm + L_SH + 9216 + r8 * 1040 + chunk * 16);
        __builtin_amdgcn_raw_buffer_store_b128(
            v, rws, (((8 * t + (j >> 1)) * 16 + (chunk >> 2)) * 64 + (chunk & 3) * 16 + 8 * (j & 1) + r8) * 16, OFF_Z2, 16);
        VMWAIT(0);
        BAR();
        if (tid == 0) __builtin_amdgcn_raw_buffer_store_b32(1u, rws, (16 * t + j) * 64, SM_FZ, 16);
      }
    }
    if (r == 0) {
      IDS
      const srd_t srd_w = make_srd(Wqkv, 512 * 1536 * 2);
#pragma unroll
      for (int c = 0; c < 4; ++c)
#pragma unroll
        for (int p = 0; p < 3; ++p)
          dma16(srd_w, wvoff, (unsigned)((8 * c + w) * 16 * 3072 + p * 1024 + j * 64), L_W + p * 32768 + (8 * c + w) * 1024);
    }
    STAMP(1 + r, 8);
    if (role == 1) {
      IDS
      unsigned LAS* scr = (unsigned LAS*)(sm + L_SH + 8704);
      if (tid == 0) scr[8] = 0u;
      v4u kv;
      for (unsigned spins = 0; spins < SPIN; ++spins) {
        kv = __builtin_amdgcn_raw_buffer_load_b128(rws, tid * 16, SM_R + r * 8192, 16);
        if (__all(valid4(kv))) break;
        SLEEP(2);
      }
      unsigned m = max(max(kv[0], kv[1]), max(kv[2], kv[3]));
      m = wave_max(m);
      if (l == 0) scr[w] = m;
      BAR();
      STAMP(1 + r, 9);
      const v4u m0 = *(const v4u LAS*)scr, m1 = *(const v4u LAS*)(scr + 4);
      const unsigned T0 = min(min(min(m0[0], m0[1]), min(m0[2], m0[3])), min(min(m1[0], m1[1]), min(m1[2], m1[3])));
      unsigned long long bm[4];
      int tot = 0;
#pragma unroll
      for (int e = 0; e < 4; ++e) { bm[e] = __ballot(kv[e] >= T0); tot += __popcll(bm[e]); }
      unsigned base = 0;
      if (l == 0) base = __hip_atomic_fetch_add(scr + 8, (unsigned)tot, __ATOMIC_RELAXED, __HIP_MEMORY_SCOPE_WORKGROUP);
      base = __builtin_amdgcn_readfirstlane(base);
#pragma unroll
      for (int e = 0; e < 4; ++e) {
        const unsigned pos = base + __builtin_amdgcn_mbcnt_hi((unsigned)(bm[e] >> 32), __builtin_amdgcn_mbcnt_lo((unsigned)bm[e], 0u));
        if (kv[e] >= T0) scr[256 + pos] = kv[e];
        base += __popcll(bm[e]);
      }
      BAR();
      const int n = (int)scr[8];
      for (int c0 = 0; c0 < n; c0 += 64) {
        const unsigned mine = c0 + l < n ? scr[256 + c0 + l] : 0u;
        int rank = 0;
        for (int i = 0; i < n; ++i) rank += scr[256 + i] > mine ? 1 : 0;
        if (c0 + l < n && rank < 8) scr[16 + 8 * w + rank] = mine;
      }
      asm volatile("s_waitcnt lgkmcnt(0)" ::: "memory");
      const unsigned mykey = scr[16 + 8 * w + (l & 7)];
      const int st = 0xfffe - (int)(mykey & 0xffffu);
      if (gu_u == 0 && w == 0) __builtin_amdgcn_raw_buffer_store_b32(mykey, rws, (l >> 3) * 64 + (l & 7) * 4, SM_IDX + r * 512, 16);
      STAMP(1 + r, 10);
      v4u xa[2], aa[2];
      v2u sv;
      const unsigned ro = (unsigned)(st * 1024 + 128 * w + 16 * g4);
      for (unsigned spins = 0; spins < SPIN; ++spins) {
#pragma unroll
        for (int i = 0; i < 2; ++i) {
          xa[i] = r == 0 ? *(const v4u*)((const char*)X + ro + 64 * i)
                         : __builtin_amdgcn_raw_buffer_load_b128(rws, ro + 64 * i, OFF_X2, 16);
          aa[i] = __builtin_amdgcn_raw_buffer_load_b128(rws, ro + 64 * i, OFF_AA + r * (2u << 20), 16);
        }
        sv = __builtin_amdgcn_raw_buffer_load_b64(rws, st * 8, SM_ST + r * 16384, 16);
        if (__all(valid4(aa[0]) && valid4(aa[1]) && sv[0] != SENT && sv[1] != SENT)) break;
        SLEEP(2);
      }
      STAMP(1 + r, 11);
      const float mb = __uint_as_float(sv[0]), ri = __uint_as_float(sv[1]);
      f32x4 ag = {0, 0, 0, 0}, au = {0, 0, 0, 0};
      const char LAS* fp = sm + L_F + 2048 * w + (8 * g4 + qd) * 32 + 8 * pp;
#pragma unroll
      for (int i = 0; i < 2; ++i) {
        float x1[8], a[8];
        unpack8(xa[i], x1);
        unpack8(aa[i], a);
#pragma unroll
        for (int e = 0; e < 8; ++e) x1[e] = rbf(rbf(x1[e] + a[e]) - mb) * ri;
        const v4u y = pack8(x1);
        ag = mfma(tr2(fp + i * 1024, 128), y, ag);
        au = mfma(tr2(fp + 16384 + i * 1024, 128), y, au);
      }
      f32x4 LAS* red = (f32x4 LAS*)(sm + L_SH + 20480) + g4 * 8 + (l & 7);
      if (tok < 8) { red[w * 64] = ag; red[w * 64 + 32] = au; }
      BAR();
      if (w == 0 && tok < 8) {
        f32x4 gt = red[0], up = red[32];
#pragma unroll
        for (int i = 1; i < 8; ++i) { gt += red[i * 64]; up += red[i * 64 + 32]; }
        float h[4];
#pragma unroll
        for (int e = 0; e < 4; ++e) {
          const float gv = rbf(gt[e]);
          h[e] = rbf(gv / (1.0f + __expf(-gv))) * rbf(up[e]);
        }
        const v2u o = {pack2(h[0], h[1]), pack2(h[2], h[3])};
        __builtin_amdgcn_raw_buffer_store_b64(o, rws, gu_u * 256 + (g4 >> 1) * 128 + tok * 16 + 8 * (g4 & 1), SM_HF + r * 32768, 16);
      }
      BAR();
    }
    STAMP(1 + r, 12);
    if (r == 0) {
      IDS
      {
        const int fi = l < 16 ? 16 * t + l : (t > 0 ? 16 * t - 2 + (l & 1) : 0);
        for (unsigned spins = 0; spins < SPIN; ++spins) {
          const unsigned f = __builtin_amdgcn_raw_buffer_load_b32(rws, fi * 64, SM_FZ, 16);
          if (__all(f != SENT)) break;
          SLEEP(4);
        }
      }
      v4u z[16], zh[2];
      aload16_sc1(zh[0], rws, lane16, OFF_Z2 + (rth * 16 + 2 * w) * 1024);
      aload16_sc1(zh[1], rws, lane16, OFF_Z2 + (rth * 16 + 2 * w + 1) * 1024);
#pragma unroll
      for (int s = 0; s < 16; ++s) aload16_sc1(z[s], rws, lane16, OFF_Z2 + ((8 * t + w) * 16 + s) * 1024);
      asm volatile("s_waitcnt vmcnt(16)" : "+v"(zh[0]), "+v"(zh[1]) :: "memory");
      *(v4u LAS*)(sm + L_SH + (2 * w) * 1024 + lane16) = zh[0];
      *(v4u LAS*)(sm + L_SH + (2 * w + 1) * 1024 + lane16) = zh[1];
      BAR();
      f32x4 acc[6], acch = {0, 0, 0, 0};
#pragma unroll
      for (int c = 0; c < 6; ++c) acc[c] = (f32x4){0, 0, 0, 0};
      if (w < 4) {
#pragma unroll
        for (int s = 0; s < 16; ++s)
          acch = mfma(tr2(wbh + s * 2048, 256), *(const v4u LAS*)(sm + L_SH + s * 1024 + lane16), acch);
      }
#pragma unroll
      for (int c = 0; c < 4; ++c) {
        if (c == 0) asm volatile("s_waitcnt vmcnt(12)" : "+v"(z[0]), "+v"(z[1]), "+v"(z[2]), "+v"(z[3]) :: "memory");
        else if (c == 1) asm volatile("s_waitcnt vmcnt(8)" : "+v"(z[4]), "+v"(z[5]), "+v"(z[6]), "+v"(z[7]) :: "memory");
        else if (c == 2) asm volatile("s_waitcnt vmcnt(4)" : "+v"(z[8]), "+v"(z[9]), "+v"(z[10]), "+v"(z[11]) :: "memory");
        else asm volatile("s_waitcnt vmcnt(0)" : "+v"(z[12]), "+v"(z[13]), "+v"(z[14]), "+v"(z[15]) :: "memory");
#pragma unroll
        for (int s4 = 0; s4 < 4; ++s4) {
          const int s = 4 * c + s4;
#pragma unroll
          for (int ct = 0; ct < 6; ++ct)
            acc[ct] = mfma(tr2(((ct & 1) ? wb1 : wb0) + (ct >> 1) * 32768 + s * 2048, 256), z[s], acc[ct]);
        }
      }
#pragma unroll
      for (int c = 0; c < 6; ++c) pq[c] = (v2u){pack2(acc[c][0], acc[c][1]), pack2(acc[c][2], acc[c][3])};
      ph = (v2u){pack2(acch[0], acch[1]), pack2(acch[2], acch[3])};
      BAR();
    }
    STAMP(1 + r, 13);
    unsigned skey;
    {
      IDS
      unsigned LAS* scr = (unsigned LAS*)(sm + L_SH + 28672);
      if (w == 0) {
        unsigned f;
        for (unsigned spins = 0; spins < SPIN; ++spins) {
          f = __builtin_amdgcn_raw_buffer_load_b32(rws, xcd * 64 + (l & 7) * 4, SM_IDX + r * 512, 16);
          if (__all(f != SENT)) break;
          SLEEP(4);
        }
        if (l < 8) scr[l] = f;
      }
      BAR();
      skey = scr[l & 7];
    }
    const int stok = 0xfffe - (int)(skey & 0xffffu);
    if (role == 2) {
      IDS
      v2u xa, aa, sv;
      const unsigned ro = (unsigned)(stok * 1024 + dn_u * 16 + 8 * (g4 & 1));
      for (unsigned spins = 0; spins < SPIN; ++spins) {
        xa = r == 0 ? *(const v2u*)((const char*)X + ro) : __builtin_amdgcn_raw_buffer_load_b64(rws, ro, OFF_X2, 16);
        aa = __builtin_amdgcn_raw_buffer_load_b64(rws, ro, OFF_AA + r * (2u << 20), 16);
        sv = __builtin_amdgcn_raw_buffer_load_b64(rws, stok * 8, SM_ST + r * 16384, 16);
        if (__all(aa[0] != SENT && aa[1] != SENT && sv[0] != SENT && sv[1] != SENT)) break;
        SLEEP(2);
      }
      v4u hf[7];
      for (unsigned spins = 0; spins < SPIN; ++spins) {
        bool ok = true;
#pragma unroll
        for (int s = 0; s < 7; ++s) {
          hf[s] = __builtin_amdgcn_raw_buffer_load_b128(rws, (7 * w + s) * 512 + g4 * 128 + (l & 7) * 16, SM_HF + r * 32768, 16);
          ok = ok && valid4(hf[s]);
        }
        if (__all(ok)) break;
        SLEEP(2);
      }
      STAMP(1 + r, 14);
      f32x4 acc = {0, 0, 0, 0};
      const char LAS* fp = sm + L_F + 224 * 16 * w + (8 * g4 + qd) * 16 + 8 * (pp & 1);
#pragma unroll
      for (int s = 0; s < 7; ++s) acc = mfma(tr2(fp + s * 512, 64), hf[s], acc);
      f32x4 LAS* red = (f32x4 LAS*)(sm + L_SH + 16384);
      red[w * 64 + l] = acc;
      BAR();
      if (w == 0 && tok < 8 && g4 < 2) {
        f32x4 y = red[l];
#pragma unroll
        for (int i = 1; i < 8; ++i) y += red[i * 64 + l];
        unsigned u = skey >> 16;
        u = (u & 0x8000u) ? (u ^ 0x8000u) : (u ^ 0xffffu);
        const float rr = __uint_as_float(u << 16);
        const float gt = rbf(1.0f / (1.0f + __expf(-rr)));
        const float mb = __uint_as_float(sv[0]), ri = __uint_as_float(sv[1]);
        const float xs[4] = {bf_lo(xa[0]), bf_hi(xa[0]), bf_lo(xa[1]), bf_hi(xa[1])};
        const float as[4] = {bf_lo(aa[0]), bf_hi(aa[0]), bf_lo(aa[1]), bf_hi(aa[1])};
        float zz[4];
#pragma unroll
        for (int e = 0; e < 4; ++e) {
          const float x1 = rbf(xs[e] + as[e]);
          const float y1 = rbf(rbf(x1 - mb) * ri);
          zz[e] = x1 + rbf(y1 + rbf(rbf(y[e]) * gt));
        }
        const v2u o = {pack2(zz[0], zz[1]), pack2(zz[2], zz[3])};
        if (r == 0) __builtin_amdgcn_raw_buffer_store_b64(o, rws, tok * 1024 + dn_u * 16 + 8 * g4, SM_X2S, 16);
        else *(v2u*)((char*)Out + ro) = o;
      }
      BAR();
    }
    IDS
    const unsigned long long own = __ballot(stok == myrow);
    if (r == 0) {
      const int lo = 128 * t - 16;
      const bool inr = stok >= lo && stok < lo + 144;
      const unsigned inmask = (unsigned)__ballot(inr) & 0xffu;
      if (inmask) {
        if ((inmask >> w) & 1) {
          v4u xv;
          for (unsigned spins = 0; spins < SPIN; ++spins) {
            xv = __builtin_amdgcn_raw_buffer_load_b128(rws, lane16, SM_X2S + w * 1024, 16);
            if (__all(valid4(xv))) break;
            SLEEP(2);
          }
          float x[8], y[8], mb, ri;
          unpack8(xv, x);
          ln_row(x, y, mb, ri);
          *(v4u LAS*)(sm + L_SH + l * 128 + w * 16) = pack8(y);
        }
        BAR();
        if (w < 6) {
          f32x4 acc = {0, 0, 0, 0};
          const char LAS* wp = ((w & 1) ? wb1 : wb0) + (w >> 1) * 32768;
#pragma unroll
          for (int s = 0; s < 16; ++s)
            acc = mfma(tr2(wp + s * 2048, 256), *(const v4u LAS*)(sm + L_SH + (4 * s + g4) * 128 + (l & 7) * 16), acc);
          pf = (v2u){pack2(acc[0], acc[1]), pack2(acc[2], acc[3])};
          patch = inr && tok < 8;
          patch_row = stok - lo;
        }
#pragma unroll
        for (int i = 0; i < 8; ++i) {
          const int ti = __builtin_amdgcn_readlane(stok, i);
          skip_main = skip_main || ti == 128 * t + 16 * w + tok;
          skip_halo = skip_halo || ti == lo + tok;
        }
        if (own) {
          const int slot = __builtin_ctzll(own);
          for (unsigned spins = 0; spins < SPIN; ++spins) {
            xres = __builtin_amdgcn_raw_buffer_load_b128(rws, lane16, SM_X2S + slot * 1024, 16);
            if (__all(valid4(xres))) break;
            SLEEP(2);
          }
          __builtin_amdgcn_raw_buffer_store_b128(xres, rws, lane16, OFF_X2 + myrow * 1024, 16);
        }
      }
    } else if (!own) {
      *(v4u*)((char*)Out + myrow * 1024 + lane16) = xo;
    }
    STAMP(1 + r, 15);
  }
}

extern "C" void kernel_launch(void* const* d_in, const int* in_sizes, int n_in, void* d_out, int out_size,
                              void* d_ws, size_t ws_size, hipStream_t stream) {
  const u16* x = (const u16*)d_in[0];
  const u16* wqkv = (const u16*)d_in[1];
  const u16* wo = (const u16*)d_in[2];
  const u16* wr = (const u16*)d_in[4];
  const u16* wg = (const u16*)d_in[5];
  const u16* wu = (const u16*)d_in[6];
  const u16* wd = (const u16*)d_in[7];
  (void)hipFuncSetAttribute((const void*)k_main, hipFuncAttributeMaxDynamicSharedMemorySize, L_TOTAL);
  k_prep<<<256, 512, 0, stream>>>(x, wqkv, wo, wg, wu, wd, (char*)d_ws);
  k_main<<<256, 512, L_TOTAL, stream>>>(x, wqkv, wo, wr, wg, wu, wd, (u16*)d_out, (char*)d_ws);
}
```

```cpp
#include <hip/hip_runtime.h>
#include <hip/hip_bf16.h>
#include <stdint.h>

#define LAS __attribute__((address_space(3)))
typedef unsigned short u16;
typedef short v4s __attribute__((ext_vector_type(4)));
typedef short v8s __attribute__((ext_vector_type(8)));
typedef __bf16 v2bf __attribute__((ext_vector_type(2)));
typedef float f32x4 __attribute__((ext_vector_type(4)));
typedef float f32x2 __attribute__((ext_vector_type(2)));
typedef unsigned v4u __attribute__((ext_vector_type(4)));
typedef unsigned v2u __attribute__((ext_vector_type(2)));

#define STAMP(k, i)
#define TS(v)
#define TSPUT(k, i, v)

constexpr int S = 2048, D = 512, F = 1792;
constexpr unsigned SENT = 0xFFFFFFFFu;
constexpr unsigned SPIN = 1u << 18;
constexpr unsigned OFF_Z1 = 0, OFF_Z2 = 2u << 20, OFF_OF = 4u << 20, OFF_AA = 8u << 20, OFF_X2 = 12u << 20,
                   OFF_SM = 14u << 20, OFF_WF = 16u << 20, OFF_WOF = 18u << 20, OFF_GUF = 19u << 20,
                   OFF_DNF = 23u << 20, WS_BYTES = 25u << 20;
constexpr unsigned SM_REC = OFF_SM, SM_HF = OFF_SM + 65536, SM_X2S = OFF_SM + 131072, SM_IDX = OFF_SM + 139264;
constexpr unsigned FL_A = OFF_SM + 262144;
constexpr unsigned FL_B = FL_A + 32768;
constexpr unsigned FL_C = FL_B + 4096;
constexpr unsigned FL_E = FL_C + 4096;
constexpr int L_W = 0, L_SH = 98304, L_OS = 131072, L_TOTAL = 163840;
constexpr int PLANE = 2304;

__device__ __forceinline__ float bf_lo(unsigned u) { return __uint_as_float(u << 16); }
__device__ __forceinline__ float bf_hi(unsigned u) { return __uint_as_float(u & 0xffff0000u); }
__device__ __forceinline__ unsigned pack2(f32x2 f) {
  v2bf c = __builtin_convertvector(f, v2bf);
  return __builtin_bit_cast(unsigned, c);
}
__device__ __forceinline__ unsigned pack2(float a, float b) { return pack2((f32x2){a, b}); }
__device__ __forceinline__ f32x2 unp(unsigned u) { return (f32x2){bf_lo(u), bf_hi(u)}; }
__device__ __forceinline__ float rbf(float a) {
  __bf16 c = (__bf16)a;
  return __uint_as_float(((unsigned)__builtin_bit_cast(u16, c)) << 16);
}
__device__ __forceinline__ float dot2(unsigned a, unsigned b, float c) {
  return __builtin_amdgcn_fdot2_f32_bf16(__builtin_bit_cast(v2bf, a), __builtin_bit_cast(v2bf, b), c, false);
}
__device__ __forceinline__ f32x4 mfma(v4u a, v4u b, f32x4 c) {
  return __builtin_amdgcn_mfma_f32_16x16x32_bf16(__builtin_bit_cast(v8s, a), __builtin_bit_cast(v8s, b), c, 0, 0, 0);
}
__device__ __forceinline__ v2u packacc(f32x4 a) { return (v2u){pack2(a[0], a[1]), pack2(a[2], a[3])}; }
typedef __amdgpu_buffer_rsrc_t srd_t;
__device__ __forceinline__ srd_t make_srd(const void* p, unsigned bytes) {
  return __builtin_amdgcn_make_buffer_rsrc((void*)p, 0, bytes, 0x00020000);
}
__device__ __forceinline__ void dma16(srd_t srd, unsigned voff, unsigned soff, unsigned lds) {
  unsigned keep;
  asm volatile("s_mov_b32 %0, m0\n\ts_mov_b32 m0, %3\n\ts_nop 0\n\tbuffer_load_dwordx4 %1, %2, %4 offen lds\n\ts_mov_b32 m0, %0"
               : "=&s"(keep) : "v"(voff), "s"(srd), "s"(lds), "s"(soff));
}
__device__ __forceinline__ void aload16(v4u& d, srd_t srd, unsigned voff, unsigned soff) {
  asm volatile("buffer_load_dwordx4 %0, %1, %2, %3 offen" : "=v"(d) : "v"(voff), "s"(srd), "s"(soff));
}
__device__ __forceinline__ void aload16_sc1(v4u& d, srd_t srd, unsigned voff, unsigned soff) {
  asm volatile("buffer_load_dwordx4 %0, %1, %2, %3 offen sc1" : "=v"(d) : "v"(voff), "s"(srd), "s"(soff));
}
__device__ __forceinline__ void astore16_sc1(v4u d, srd_t srd, unsigned voff, unsigned soff) {
  asm volatile("buffer_store_dwordx4 %0, %1, %2, %3 offen sc1\n\ts_nop 1" :: "v"(d), "v"(voff), "s"(srd), "s"(soff));
}
#define SLEEP(n) do { __builtin_amdgcn_s_sleep(n); asm volatile("" ::: "memory"); } while (0)
#define VMWAIT(n) asm volatile("s_waitcnt vmcnt(" #n ")" ::: "memory")
#define BAR() do { asm volatile("s_waitcnt lgkmcnt(0)" ::: "memory"); __builtin_amdgcn_s_barrier(); asm volatile("" ::: "memory"); } while (0)
#define LD128(voff, soff) __builtin_amdgcn_raw_buffer_load_b128(rws, voff, soff, 16)
__device__ __forceinline__ void st128(v4u d, srd_t srd, unsigned voff, unsigned soff) {
  asm volatile("buffer_store_dwordx4 %0, %1, %2, %3 offen sc1\n\ts_nop 1" :: "v"(d), "v"(voff), "s"(srd), "s"(soff) : "memory");
}
#define ST128(v, voff, soff) st128(v, rws, voff, soff)
__device__ __forceinline__ unsigned umax4(v4u v) { return max(max(v[0], v[1]), max(v[2], v[3])); }
__device__ __forceinline__ bool valid4(v4u v) { return umax4(v) != SENT; }

template <int CTRL, int RMASK>
__device__ __forceinline__ float dpp_add(float v) {
  int t = __builtin_amdgcn_update_dpp(0, __float_as_int(v), CTRL, RMASK, 0xf, true);
  return v + __int_as_float(t);
}
__device__ __forceinline__ float wave_sum(float v) {
  v = dpp_add<0x111, 0xf>(v); v = dpp_add<0x112, 0xf>(v); v = dpp_add<0x114, 0xf>(v); v = dpp_add<0x118, 0xf>(v);
  v = dpp_add<0x142, 0xa>(v); v = dpp_add<0x143, 0xc>(v);
  return __int_as_float(__builtin_amdgcn_readlane(__float_as_int(v), 63));
}
template <int CTRL, int RMASK>
__device__ __forceinline__ unsigned dpp_max(unsigned v) {
  unsigned t = (unsigned)__builtin_amdgcn_update_dpp(0, (int)v, CTRL, RMASK, 0xf, true);
  return v > t ? v : t;
}
__device__ __forceinline__ unsigned wave_max(unsigned v) {
  v = dpp_max<0x111, 0xf>(v); v = dpp_max<0x112, 0xf>(v); v = dpp_max<0x114, 0xf>(v); v = dpp_max<0x118, 0xf>(v);
  v = dpp_max<0x142, 0xa>(v); v = dpp_max<0x143, 0xc>(v);
  return (unsigned)__builtin_amdgcn_readlane((int)v, 63);
}
__device__ __forceinline__ unsigned norm2(f32x2 x, float mb, float ri) { return pack2(unp(pack2(x - mb)) * ri); }
__device__ __forceinline__ v4u ln_row(const f32x2 (&x)[4], float& mb, float& ri) {
  const float c = __int_as_float(__builtin_amdgcn_readfirstlane(__float_as_int(x[0][0])));
  const f32x2 d0 = x[0] - c, d1 = x[1] - c, d2 = x[2] - c, d3 = x[3] - c;
  const f32x2 s2 = (d0 + d1) + (d2 + d3);
  const f32x2 q2 = (d0 * d0 + d1 * d1) + (d2 * d2 + d3 * d3);
  float s = s2[0] + s2[1], q = q2[0] + q2[1];
  s = dpp_add<0x111, 0xf>(s); q = dpp_add<0x111, 0xf>(q);
  s = dpp_add<0x112, 0xf>(s); q = dpp_add<0x112, 0xf>(q);
  s = dpp_add<0x114, 0xf>(s); q = dpp_add<0x114, 0xf>(q);
  s = dpp_add<0x118, 0xf>(s); q = dpp_add<0x118, 0xf>(q);
  s = dpp_add<0x142, 0xa>(s); q = dpp_add<0x142, 0xa>(q);
  s = dpp_add<0x143, 0xc>(s); q = dpp_add<0x143, 0xc>(q);
  const float dm = __int_as_float(__builtin_amdgcn_readlane(__float_as_int(s), 63)) * (1.0f / D);
  const float var = fmaxf(__int_as_float(__builtin_amdgcn_readlane(__float_as_int(q), 63)) * (1.0f / D) - dm * dm, 0.f);
  mb = rbf(c + dm);
  const float sg = rbf(__builtin_amdgcn_sqrtf(rbf(rbf(var) + 1e-5f)));
  ri = __builtin_amdgcn_rcpf(sg);
  return (v4u){norm2(x[0], mb, ri), norm2(x[1], mb, ri), norm2(x[2], mb, ri), norm2(x[3], mb, ri)};
}

#define IDS                                                                                              \
  int tid = threadIdx.x, w = w0, b = blockIdx.x;                                                         \
  asm volatile("" : "+v"(tid)); asm volatile("" : "+s"(w)); asm volatile("" : "+s"(b));                  \
  const int l = tid & 63, xcd = b & 7, jj = b >> 3;                                                      \
  const int t = 4 * (xcd >> 1) + (jj >> 3), j = 8 * (xcd & 1) + (jj & 7);                                \
  const int g4 = l >> 4, tok = l & 15;                                                                   \
  const int rq = j >> 2, cq = j & 3, myrow = 128 * t + 8 * j + w;                                        \
  const int gu_u = 14 * xcd + jj, dn_u = 4 * xcd + (jj - 14);                                            \
  const unsigned lane16 = l * 16;                                                                        \
  const int rth = t > 0 ? 8 * t - 1 : 0;                                                                 \
  (void)rq; (void)cq; (void)myrow; (void)gu_u; (void)dn_u; (void)lane16; (void)rth; (void)tok; (void)g4;

__device__ __forceinline__ void attention(char LAS* sm, srd_t rws, int w0, int r) {
  IDS
  const char LAS* rp = sm + L_SH + g4 * PLANE + (16 + 16 * w + tok) * 16;
  const v4u q0 = *(const v4u LAS*)rp;
  float d[8];
#pragma unroll
  for (int k = 0; k < 8; ++k) {
    const v4u k0 = *(const v4u LAS*)(rp + 4 * PLANE - 32 * k);
    d[k] = dot2(q0[3], k0[3], dot2(q0[2], k0[2], dot2(q0[1], k0[1], dot2(q0[0], k0[0], 0.f))));
  }
  float lg[4];
#pragma unroll
  for (int k = 0; k < 4; ++k) {
    const auto sw = __builtin_amdgcn_permlane16_swap(__float_as_uint(d[k]), __float_as_uint(d[k + 4]), false, false);
    lg[k] = rbf(__uint_as_float(sw[0]) + __uint_as_float(sw[1]));
  }
  if (t == 0) {
#pragma unroll
    for (int k = 0; k < 4; ++k) lg[k] = (16 * w + tok - 2 * k - 8 * (g4 & 1) >= 0) ? lg[k] : -4e9f;
  }
  float m = fmaxf(fmaxf(lg[0], lg[1]), fmaxf(lg[2], lg[3]));
  {
    const auto sw = __builtin_amdgcn_permlane16_swap(__float_as_uint(m), __float_as_uint(m), false, false);
    m = fmaxf(__uint_as_float(sw[0]), __uint_as_float(sw[1]));
  }
  constexpr float C = 0.25f * 1.44269504088896341f;
#pragma unroll
  for (int k = 0; k < 4; ++k) lg[k] = __builtin_amdgcn_exp2f((lg[k] - m) * C);
  float sum = (lg[0] + lg[1]) + (lg[2] + lg[3]);
  {
    const auto sw = __builtin_amdgcn_permlane16_swap(__float_as_uint(sum), __float_as_uint(sum), false, false);
    sum = __uint_as_float(sw[0]) + __uint_as_float(sw[1]);
  }
  const float inv = __builtin_amdgcn_rcpf(sum);
  const unsigned pa = pack2(lg[0] * inv, lg[1] * inv), pb = pack2(lg[2] * inv, lg[3] * inv);
  const auto sa = __builtin_amdgcn_permlane16_swap(pa, pa, false, false);
  const auto sb = __builtin_amdgcn_permlane16_swap(pb, pb, false, false);
  const unsigned pp[4] = {sa[0], sb[0], sa[1], sb[1]};
  float o[8];
#pragma unroll
  for (int e = 0; e < 8; ++e) o[e] = 0.f;
#pragma unroll
  for (int k = 0; k < 4; ++k) {
    const v4u v0 = *(const v4u LAS*)(rp + 8 * PLANE - 64 * k), v1 = *(const v4u LAS*)(rp + 8 * PLANE - 64 * k - 32);
#pragma unroll
    for (int e = 0; e < 4; ++e) {
      o[2 * e] = dot2(pp[k], __builtin_amdgcn_perm(v1[e], v0[e], 0x05040100u), o[2 * e]);
      o[2 * e + 1] = dot2(pp[k], __builtin_amdgcn_perm(v1[e], v0[e], 0x07060302u), o[2 * e + 1]);
    }
  }
  const v4u ov = {pack2(o[0], o[1]), pack2(o[2], o[3]), pack2(o[4], o[5]), pack2(o[6], o[7])};
  VMWAIT(0);
  ST128(ov, lane16, OFF_OF + r * (2u << 20) + ((8 * t + w) * 16 + j) * 1024);
}

struct Unit { srd_t srd; unsigned stride, dst, dstep, dodd; };
__device__ __forceinline__ Unit ffn_unit(int u, const u16* Wg, const u16* Wu, const u16* Wd) {
  Unit r;
  const char* src = (const char*)Wd;
  r.stride = 1024; r.dst = WS_BYTES; r.dstep = 0; r.dodd = 0;
  if (u < 896) {
    const int up = u >= 448, v = u - 448 * up, s = v / 28, nb = v % 28;
    src = (const char*)(up ? Wu : Wg) + (size_t)(32 * s) * (F * 2) + nb * 128; r.stride = F * 2;
    r.dst = OFF_GUF + ((4 * nb * 16 + s) * 2 + up) * 1024; r.dstep = 2 * 32 * 1024; r.dodd = 32 * 1024;
  } else if (u < 1344) {
    const int s = (u - 896) >> 3, nb = u & 7;
    src = (const char*)Wd + (size_t)(32 * s) * 1024 + nb * 128;
    r.dst = OFF_DNF + (4 * nb * 56 + s) * 1024; r.dstep = 2 * 56 * 1024; r.dodd = 56 * 1024;
  }
  r.srd = make_srd(src, u < 1344 ? 32 * F * 2 : 0);
  return r;
}
__device__ __forceinline__ void unit_store(char LAS* scr, srd_t rws, int l, const v4u (&wv)[4], unsigned dst, unsigned dstep, unsigned dodd) {
#pragma unroll
  for (int i = 0; i < 4; ++i) *(v4u LAS*)(scr + i * 1024 + l * 16) = wv[i];
  asm volatile("s_waitcnt lgkmcnt(0)" ::: "memory");
  const char LAS* p = scr + (8 * (l >> 4) + ((l & 15) >> 2)) * 128 + 8 * (l & 3);
  v4s a[4], c[4];
#pragma unroll
  for (int i = 0; i < 4; ++i) {
    a[i] = __builtin_amdgcn_ds_read_tr16_b64_v4i16((LAS v4s*)(p + 32 * i));
    c[i] = __builtin_amdgcn_ds_read_tr16_b64_v4i16((LAS v4s*)(p + 32 * i + 512));
  }
#pragma unroll
  for (int i = 0; i < 4; ++i) {
    const v8s f = {a[i][0], a[i][1], a[i][2], a[i][3], c[i][0], c[i][1], c[i][2], c[i][3]};
    astore16_sc1(__builtin_bit_cast(v4u, f), rws, l * 16, dst + (i >> 1) * dstep + (i & 1) * dodd);
  }
  asm volatile("" ::: "memory");
}
__device__ __forceinline__ void wait_all_flags(srd_t rws, int l, unsigned off, unsigned lo, unsigned hi) {
  _Pragma("unroll 1") for (unsigned spins = 0; spins < SPIN; ++spins) {
    const v4u a = LD128(l * 32, off), c = LD128(l * 32 + 16, off);
    if (__all((((a[0] ^ lo) | (a[1] ^ hi)) | ((a[2] ^ lo) | (a[3] ^ hi)) | ((c[0] ^ lo) | (c[1] ^ hi)) | ((c[2] ^ lo) | (c[3] ^ hi))) == 0u)) break;
    SLEEP(3);
  }
}
constexpr int L_CTL = L_SH + 32704;
#define WFRAG(s, ct) (((ct) >> 1) * 32768 + (s) * 2048 + ((ct) & 1) * 1024)

template <int R>
__device__ __forceinline__ void qkv_gemm(char LAS* sm, srd_t rws, int w0, unsigned tag_lo, unsigned tag_hi,
                                         const u16* Wr, const u16* Wg, const u16* Wu, const u16* Wd, v4u& wrv,
                                         v4u (&wof)[16], v4u (&wv)[4]) {
  IDS
  {
    const int fi = l < 16 ? 16 * t + l : (t > 0 ? 16 * t - 2 + (l & 1) : 0);
    _Pragma("unroll 1") for (unsigned spins = 0; spins < SPIN; ++spins) {
      const v2u f = __builtin_amdgcn_raw_buffer_load_b64(rws, fi * 64, FL_A + R * 16384, 16);
      if (__all(((f[0] ^ (tag_lo + R)) | (f[1] ^ tag_hi)) == 0u)) break;
      SLEEP(3);
    }
  }
  STAMP(3, 4 + 4 * R);
  v4u z[16], zh[2];
  const unsigned zo = R ? OFF_Z2 : OFF_Z1, zb = zo + (8 * t + w) * 16384;
  aload16_sc1(zh[0], rws, lane16, zo + (rth * 16 + 2 * w) * 1024);
  aload16_sc1(zh[1], rws, lane16, zo + (rth * 16 + 2 * w + 1) * 1024);
#pragma unroll
  for (int s = 0; s < 8; ++s) aload16_sc1(z[s], rws, lane16, zb + s * 1024);
  const char LAS* wp = sm + L_W + lane16;
  const char LAS* wh = wp + (1 + ((w >> 1) & 1)) * 32768 + (w & 1) * 1024 + (w >> 2) * 2048;
  const char LAS* hp = sm + L_OS + lane16 + (w >> 2) * 1024;
  v4u hw, hz;
  v4u f[2][6];
#pragma unroll
  for (int ct = 0; ct < 6; ++ct) f[0][ct] = *(const v4u LAS*)(wp + WFRAG(0, ct));
  asm volatile("s_waitcnt vmcnt(8)" : "+v"(zh[0]), "+v"(zh[1]) :: "memory");
  *(v4u LAS*)(sm + L_OS + (2 * w) * 1024 + lane16) = zh[0];
  *(v4u LAS*)(sm + L_OS + (2 * w + 1) * 1024 + lane16) = zh[1];
  BAR();
  f32x4 acc[6], acch = {0, 0, 0, 0};
#pragma unroll
  for (int c = 0; c < 6; ++c) acc[c] = (f32x4){0, 0, 0, 0};
  const unsigned wob = OFF_WOF + (8 * cq + w) * 16384;
#pragma unroll
  for (int c = 0; c < 4; ++c) {
    if (c == 0) asm volatile("s_waitcnt vmcnt(4)" : "+v"(z[0]), "+v"(z[1]), "+v"(z[2]), "+v"(z[3]) :: "memory");
    else if (c == 1) asm volatile("s_waitcnt vmcnt(4)" : "+v"(z[4]), "+v"(z[5]), "+v"(z[6]), "+v"(z[7]) :: "memory");
    else if (c == 2) asm volatile("s_waitcnt vmcnt(4)" : "+v"(z[8]), "+v"(z[9]), "+v"(z[10]), "+v"(z[11]) :: "memory");
    else if (R == 0) asm volatile("s_waitcnt vmcnt(12)" : "+v"(z[12]), "+v"(z[13]), "+v"(z[14]), "+v"(z[15]) :: "memory");
    else asm volatile("s_waitcnt vmcnt(0)" : "+v"(z[12]), "+v"(z[13]), "+v"(z[14]), "+v"(z[15]) :: "memory");
    if (R == 0 && c == 2) {
      volatile unsigned LAS* ok = (volatile unsigned LAS*)(sm + L_CTL) + 2;
      if (w == 7) {
        wait_all_flags(rws, l, FL_B, tag_lo + 2, tag_hi);
        if (l == 0) *ok = 1u;
      } else {
        _Pragma("unroll 1") for (unsigned spins = 0; spins < (SPIN << 4) && !*ok; ++spins) SLEEP(1);
      }
      asm volatile("" ::: "memory");
    }
#pragma unroll
    for (int s4 = 0; s4 < 4; ++s4) {
      const int s = 4 * c + s4;
      if (s < 15) {
#pragma unroll
        for (int ct = 0; ct < 6; ++ct) f[(s + 1) & 1][ct] = *(const v4u LAS*)(wp + WFRAG(s + 1, ct));
      }
      if (!(s & 1)) { hw = *(const v4u LAS*)(wh + s * 2048); hz = *(const v4u LAS*)(hp + s * 1024); }
      __builtin_amdgcn_sched_barrier(0);
#pragma unroll
      for (int ct = 0; ct < 6; ++ct) acc[ct] = mfma(f[s & 1][ct], z[s], acc[ct]);
      if (s & 1) acch = mfma(hw, hz, acch);
      if (c < 2) aload16_sc1(z[4 * (c + 2) + s4], rws, lane16, zb + (4 * (c + 2) + s4) * 1024);
      else if (R == 0 && c == 2) {
#pragma unroll
        for (int i = 0; i < 3; ++i) aload16_sc1(wof[3 * s4 + i], rws, lane16, wob + (3 * s4 + i) * 1024);
      } else if (R == 0) {
        aload16_sc1(wof[12 + s4], rws, lane16, wob + (12 + s4) * 1024);
        const Unit un = ffn_unit(w * 256 + b, Wg, Wu, Wd);
        aload16(wv[s4], un.srd, (unsigned)(l >> 3) * un.stride + (l & 7) * 16, 8 * s4 * un.stride);
        if (s4 == 3) aload16(wrv, make_srd(Wr, D * 2), lane16, 0);
      }
      __builtin_amdgcn_sched_barrier(0);
    }
  }
  char LAS* qp = sm + L_SH + (g4 >> 1) * PLANE + 8 * (g4 & 1);
#pragma unroll
  for (int c = 0; c < 6; ++c) *(v2u LAS*)(qp + 2 * c * PLANE + (16 + 16 * w + tok) * 16) = packacc(acc[c]);
  f32x4 LAS* hx = (f32x4 LAS*)(sm + L_OS + 16384 + (w & 3) * 1024 + lane16);
  if (w >= 4) *hx = acch;
  BAR();
  if (w < 4) *(v2u LAS*)(qp + 2 * (2 + w) * PLANE + tok * 16) = packacc(acch + *hx);
}

__global__ __launch_bounds__(512, 2) void k_main(const u16* __restrict__ X, const u16* __restrict__ Wqkv,
                                                 const u16* __restrict__ Wo, const u16* __restrict__ Wr,
                                                 const u16* __restrict__ Wg, const u16* __restrict__ Wu,
                                                 const u16* __restrict__ Wd, u16* __restrict__ Out,
                                                 char* __restrict__ ws) {
  extern __shared__ __attribute__((aligned(16))) char smem[];
  char LAS* sm = (char LAS*)smem;
  const int w0 = __builtin_amdgcn_readfirstlane(threadIdx.x >> 6);
  const srd_t rws = make_srd(ws, WS_BYTES);
  const int role = (blockIdx.x >> 3) < 14 ? 1 : (blockIdx.x >> 3) < 18 ? 2 : 0;
  STAMP(1, 0);
  v4u xres, wrv;
  v4u wof[16];
  unsigned tag_lo, tag_hi, ep_lo, ep_hi;
  v4u wv[4];
  {
    IDS
    v2u ev;
    asm volatile("buffer_load_dwordx2 %0, off, %1, %2 sc1" : "=v"(ev) : "s"(rws), "s"(FL_E));
    aload16(xres, make_srd(X, S * D * 2), lane16, myrow * 1024);
    if (tid < 4) ((unsigned LAS*)(sm + L_CTL))[tid] = 0u;
    const v4u sv = {SENT, SENT, SENT, SENT};
    const bool wo_unit = w == 5 && b >= 128;
    {
      const srd_t su = make_srd((const char*)Wo + (size_t)(32 * ((b >> 3) & 15)) * 1024 + (b & 7) * 128, wo_unit ? 32768 : 0);
#pragma unroll
      for (int i = 0; i < 4; ++i) aload16(wv[i], su, (unsigned)(l >> 3) * 1024 + (l & 7) * 16, 8 * i * 1024);
    }
    if (w < 6) {
      const srd_t srd_w = make_srd(Wqkv, 512 * 1536 * 2);
      const unsigned wvoff = (unsigned)((l >> 2) * 3072 + (((l & 3) ^ (((l >> 5) & 1) << 1)) * 16));
      const unsigned wso = (unsigned)((w & 1) * 256 * 3072 + (w >> 1) * 1024 + j * 64);
#pragma unroll
      for (int i = 0; i < 16; ++i) dma16(srd_w, wvoff, wso + i * 16 * 3072, L_W + w * 16384 + i * 1024);
      const int w2 = 2 * (w & 3);
      const unsigned ao = (unsigned)((128 * t + 32 * rq + 4 * w2 + g4) * 1024 + cq * 256 + tok * 16);
      const unsigned lim = w < 4 ? 0u : WS_BYTES;
      astore16_sc1(sv, rws, lane16, lim + OFF_OF + ((8 * t + w2) * 16 + j) * 1024);
      astore16_sc1(sv, rws, lane16, lim + OFF_OF + ((8 * t + w2 + 1) * 16 + j) * 1024);
      astore16_sc1(sv, rws, ao, lim + OFF_AA);
      astore16_sc1(sv, rws, l < 36 || w < 4 ? (w < 4 ? ao : lane16) : WS_BYTES, w < 4 ? OFF_AA + 4096 : (w == 4 ? OFF_SM + b * 576 : WS_BYTES));
      VMWAIT(24);
    } else {
      VMWAIT(0);
    }
    asm volatile("" : "+v"(ev), "+v"(xres));
    STAMP(3, 0);
    ep_lo = __builtin_amdgcn_readfirstlane(ev[0]);
    ep_hi = __builtin_amdgcn_readfirstlane(ev[1]);
    {
      const unsigned long long e = ((unsigned long long)ep_hi << 32) | ep_lo;
      const unsigned long long tg = (e + 1) * 0x9E3779B97F4A7C15ull;
      tag_lo = (unsigned)tg & ~3u; tag_hi = (unsigned)(tg >> 32);
    }
    {
      f32x2 x[4] = {unp(xres[0]), unp(xres[1]), unp(xres[2]), unp(xres[3])};
      float mb, ri;
      *(v4u LAS*)(sm + L_OS + w * 1040 + lane16) = ln_row(x, mb, ri);
    }
    BAR();
    if (w >= 6) {
#pragma unroll
      for (int i = 0; i < 4; ++i) {
        const int chunk = 32 * (w - 6) + 8 * i + (l >> 3), r8 = l & 7;
        const v4u v = *(const v4u LAS*)(sm + L_OS + r8 * 1040 + chunk * 16);
        ST128(v, (((8 * t + (j >> 1)) * 16 + (chunk >> 2)) * 64 + (chunk & 3) * 16 + 8 * (j & 1) + r8) * 16, OFF_Z1);
      }
      VMWAIT(0);
      unsigned old = 0;
      if (l == 0) old = __hip_atomic_fetch_add((unsigned LAS*)(sm + L_CTL), 1u, __ATOMIC_RELAXED, __HIP_MEMORY_SCOPE_WORKGROUP);
      if (l == 0 && old == 1) __builtin_amdgcn_raw_buffer_store_b64((v2u){tag_lo, tag_hi}, rws, (16 * t + j) * 64, FL_A, 16);
    } else {
      asm volatile("s_waitcnt vmcnt(20)" : "+v"(wv[0]), "+v"(wv[1]), "+v"(wv[2]), "+v"(wv[3]) :: "memory");
      if (wo_unit) unit_store(sm + L_SH, rws, l, wv, OFF_WOF + (4 * (b & 7) * 16 + ((b >> 3) & 15)) * 1024, 32768, 16384);
      VMWAIT(0);
    }
    BAR();
    if (tid == 0) __builtin_amdgcn_raw_buffer_store_b64((v2u){tag_lo + 2, tag_hi}, rws, b * 8, FL_B, 16);
    STAMP(3, 2);
    {
      const int qd = (l & 15) >> 2, pp = l & 3;
      const char LAS* p0 = sm + L_W + w * 12288 + (8 * g4 + qd) * 64 + (((pp >> 1) ^ ((g4 & 1) << 1)) * 16) + 8 * (pp & 1);
      const char LAS* p1 = sm + L_W + w * 12288 + (8 * g4 + qd) * 64 + ((((pp >> 1) ^ ((g4 & 1) << 1)) ^ 2) * 16) + 8 * (pp & 1);
      v4s fr[6][4];
#pragma unroll
      for (int i = 0; i < 6; ++i) {
        fr[i][0] = __builtin_amdgcn_ds_read_tr16_b64_v4i16((LAS v4s*)(p0 + i * 2048));
        fr[i][1] = __builtin_amdgcn_ds_read_tr16_b64_v4i16((LAS v4s*)(p0 + i * 2048 + 256));
        fr[i][2] = __builtin_amdgcn_ds_read_tr16_b64_v4i16((LAS v4s*)(p1 + i * 2048));
        fr[i][3] = __builtin_amdgcn_ds_read_tr16_b64_v4i16((LAS v4s*)(p1 + i * 2048 + 256));
      }
      asm volatile("s_waitcnt lgkmcnt(0)" ::: "memory");
#pragma unroll
      for (int i = 0; i < 6; ++i) {
        const v8s a = {fr[i][0][0], fr[i][0][1], fr[i][0][2], fr[i][0][3], fr[i][1][0], fr[i][1][1], fr[i][1][2], fr[i][1][3]};
        const v8s c = {fr[i][2][0], fr[i][2][1], fr[i][2][2], fr[i][2][3], fr[i][3][0], fr[i][3][1], fr[i][3][2], fr[i][3][3]};
        *(v8s LAS*)(sm + L_W + w * 12288 + i * 2048 + lane16) = a;
        *(v8s LAS*)(sm + L_W + w * 12288 + i * 2048 + 1024 + lane16) = c;
      }
    }
    BAR();
    STAMP(3, 3);
  }
  qkv_gemm<0>(sm, rws, w0, tag_lo, tag_hi, Wr, Wg, Wu, Wd, wrv, wof, wv);
  BAR();
  STAMP(1, 2);
  attention(sm, rws, w0, 0);
  asm volatile("" : "+v"(wrv), "+v"(wof[0]), "+v"(wof[1]), "+v"(wof[2]), "+v"(wof[3]), "+v"(wof[4]), "+v"(wof[5]),
               "+v"(wof[6]), "+v"(wof[7]), "+v"(wof[8]), "+v"(wof[9]), "+v"(wof[10]), "+v"(wof[11]), "+v"(wof[12]),
               "+v"(wof[13]), "+v"(wof[14]), "+v"(wof[15]));
  asm volatile("" : "+v"(wv[0]), "+v"(wv[1]), "+v"(wv[2]), "+v"(wv[3]));

#pragma unroll 1
  for (int r = 0; r < 2; ++r) {
    STAMP(1 + r, 3);
    {
      IDS
      v4u of[4];
      const unsigned ofb = OFF_OF + r * (2u << 20) + ((8 * t + 2 * rq) * 16 + 4 * w) * 1024;
      SLEEP(10);
#pragma unroll
      for (int i = 0; i < 4; ++i) aload16_sc1(of[i], rws, lane16, ofb + i * 1024);
      if (r == 0) {
        const Unit un = ffn_unit(w * 256 + b, Wg, Wu, Wd);
        unit_store(sm + L_OS + w * 4096, rws, l, wv, un.dst, un.dstep, un.dodd);
        VMWAIT(4);
      } else {
        VMWAIT(0);
      }
      asm volatile("" : "+v"(of[0]), "+v"(of[1]), "+v"(of[2]), "+v"(of[3]));
#pragma unroll
      for (int i = 0; i < 4; ++i) wv[i] = (v4u){0u, 0u, 0u, 0u};
      _Pragma("unroll 1") for (unsigned spins = 0; spins < SPIN; ++spins) {
        if (__all(max(max(umax4(of[0]), umax4(of[1])), max(umax4(of[2]), umax4(of[3]))) != SENT)) break;
        SLEEP(2);
#pragma unroll
        for (int i = 0; i < 4; ++i) of[i] = LD128(lane16, ofb + i * 1024);
      }
#pragma unroll
      for (int i = 0; i < 4; ++i) *(v4u LAS*)(sm + L_OS + (4 * w + i) * 1024 + lane16) = of[i];
    }
    BAR();
    STAMP(1 + r, 4);
    {
      IDS
      f32x4 a0 = {0, 0, 0, 0}, a1 = {0, 0, 0, 0};
      const char LAS* op = sm + L_OS + lane16;
      v4u f[2][8];
#pragma unroll
      for (int i = 0; i < 4; ++i) { f[0][2 * i] = *(const v4u LAS*)(op + i * 1024); f[0][2 * i + 1] = *(const v4u LAS*)(op + (16 + i) * 1024); }
#pragma unroll
      for (int c = 0; c < 4; ++c) {
        if (c < 3) {
#pragma unroll
          for (int i = 0; i < 4; ++i) {
            f[(c + 1) & 1][2 * i] = *(const v4u LAS*)(op + (4 * c + 4 + i) * 1024);
            f[(c + 1) & 1][2 * i + 1] = *(const v4u LAS*)(op + (20 + 4 * c + i) * 1024);
          }
        }
        __builtin_amdgcn_sched_barrier(0);
#pragma unroll
        for (int i = 0; i < 4; ++i) {
          a0 = mfma(wof[4 * c + i], f[c & 1][2 * i], a0);
          a1 = mfma(wof[4 * c + i], f[c & 1][2 * i + 1], a1);
        }
        __builtin_amdgcn_sched_barrier(0);
      }
      char LAS* tp = sm + L_SH + tok * 272 + 32 * w + 8 * g4;
      *(v2u LAS*)tp = packacc(a0);
      *(v2u LAS*)(tp + 16 * 272) = packacc(a1);
      BAR();
      const v4u v = *(const v4u LAS*)(sm + L_SH + (4 * w + g4) * 272 + tok * 16);
      ST128(v, (128 * t + 32 * rq + 4 * w + g4) * 1024 + cq * 256 + tok * 16, OFF_AA + r * (2u << 20));
    }
    STAMP(1 + r, 5);
    v4u xo;
    {
      IDS
      SLEEP(10);
      v4u av;
      _Pragma("unroll 1") for (unsigned spins = 0; spins < SPIN; ++spins) {
        av = LD128(lane16, OFF_AA + r * (2u << 20) + myrow * 1024);
        if (__all(valid4(av))) break;
        SLEEP(2);
      }
      STAMP(1 + r, 6);
      f32x2 x1[4];
      float mb, ri;
#pragma unroll
      for (int e = 0; e < 4; ++e) x1[e] = unp(pack2(unp(xres[e]) + unp(av[e])));
      const v4u y = ln_row(x1, mb, ri);
      const float d = wave_sum(dot2(y[3], wrv[3], dot2(y[2], wrv[2], dot2(y[1], wrv[1], dot2(y[0], wrv[0], 0.f)))));
      unsigned u = __float_as_uint(rbf(d)) >> 16;
      u = (u & 0x8000u) ? (u ^ 0xffffu) : (u ^ 0x8000u);
      v4u rec = {(u << 16) | (unsigned)(0xfffe - myrow), __float_as_uint(mb), __float_as_uint(ri), 0u};
      rec[1] = rec[1] == SENT ? 0x7fc00000u : rec[1];
      rec[2] = rec[2] == SENT ? 0x7fc00000u : rec[2];
      if (l == 0) *(v4u LAS*)(sm + L_SH + 8960 + w * 16) = rec;
      BAR();
      if (tid < 8) ST128(*(const v4u LAS*)(sm + L_SH + 8960 + tid * 16), (128 * t + 8 * j + tid) * 16, SM_REC + r * 32768);
      if (r == 0 && tid == 64) __builtin_amdgcn_raw_buffer_store_b64((v2u){tag_lo + 3, tag_hi}, rws, b * 8, FL_C, 16);
      STAMP(1 + r, 7);
#pragma unroll
      for (int e = 0; e < 4; ++e) xo[e] = pack2(x1[e] + unp(y[e]));
      if (r == 0) {
        xres = xo;
        ST128(xo, lane16, OFF_X2 + myrow * 1024);
#pragma unroll
        for (int e = 0; e < 4; ++e) x1[e] = unp(xo[e]);
        *(v4u LAS*)(sm + L_SH + 9216 + w * 1040 + lane16) = ln_row(x1, mb, ri);
        BAR();
        const int chunk = 8 * w + (l >> 3), r8 = l & 7;
        const v4u v = *(const v4u LAS*)(sm + L_SH + 9216 + r8 * 1040 + chunk * 16);
        ST128(v, (((8 * t + (j >> 1)) * 16 + (chunk >> 2)) * 64 + (chunk & 3) * 16 + 8 * (j & 1) + r8) * 16, OFF_Z2);
        const v4u sv = {SENT, SENT, SENT, SENT};
        ST128(sv, lane16, OFF_OF + (2u << 20) + ((8 * t + w) * 16 + j) * 1024);
        ST128(sv, (128 * t + 32 * rq + 4 * w + g4) * 1024 + cq * 256 + tok * 16, OFF_AA + (2u << 20));
        VMWAIT(0);
        BAR();
        if (tid == 0) __builtin_amdgcn_raw_buffer_store_b64((v2u){tag_lo + 1, tag_hi}, rws, (16 * t + j) * 64, FL_A + 16384, 16);
      }
    }
    STAMP(1 + r, 8);
    if (role == 1) {
      IDS
      unsigned LAS* scr = (unsigned LAS*)(sm + L_OS);
      v4u LAS* picks = (v4u LAS*)(sm + L_OS + 1024) + 8 * w;
      v4u LAS* cand = (v4u LAS*)(sm + L_OS + 2048);
      unsigned LAS* ckey = (unsigned LAS*)(sm + L_SH + 9216);
      if (tid == 0) scr[8] = 0u;
      if (tid < 64) ckey[tid] = 0u;
      if (r == 0) wait_all_flags(rws, l, FL_C, tag_lo + 3, tag_hi);
      v4u ffw[4];
#pragma unroll
      for (int i = 0; i < 4; ++i) ffw[i] = LD128(lane16, OFF_GUF + (gu_u * 16 + 2 * w) * 2048 + i * 1024);
      v4u rc[4];
      _Pragma("unroll 1") for (unsigned spins = 0; spins < SPIN; ++spins) {
#pragma unroll
        for (int e = 0; e < 4; ++e) rc[e] = LD128(tid * 16, SM_REC + r * 32768 + e * 8192);
        if (__all(max(max(umax4(rc[0]), umax4(rc[1])), max(umax4(rc[2]), umax4(rc[3]))) != SENT)) break;
        SLEEP(4);
      }
      unsigned m = max(max(rc[0][0], rc[1][0]), max(rc[2][0], rc[3][0]));
      m = wave_max(m);
      if (l == 0) scr[w] = m;
      BAR();
      STAMP(1 + r, 9);
      const v4u m0 = *(const v4u LAS*)scr, m1 = *(const v4u LAS*)(scr + 4);
      const unsigned T0 = min(min(min(m0[0], m0[1]), min(m0[2], m0[3])), min(min(m1[0], m1[1]), min(m1[2], m1[3])));
      unsigned long long bm[4];
      int tot = 0;
#pragma unroll
      for (int e = 0; e < 4; ++e) { bm[e] = __ballot(rc[e][0] >= T0); tot += __popcll(bm[e]); }
      unsigned base = 0;
      if (l == 0) base = __hip_atomic_fetch_add(scr + 8, (unsigned)tot, __ATOMIC_RELAXED, __HIP_MEMORY_SCOPE_WORKGROUP);
      base = __builtin_amdgcn_readfirstlane(base);
#pragma unroll
      for (int e = 0; e < 4; ++e) {
        const unsigned pos = base + __builtin_amdgcn_mbcnt_hi((unsigned)(bm[e] >> 32), __builtin_amdgcn_mbcnt_lo((unsigned)bm[e], 0u));
        if (rc[e][0] >= T0) { cand[pos] = rc[e]; ckey[pos] = rc[e][0]; }
        base += __popcll(bm[e]);
      }
      BAR();
      const int n = __builtin_amdgcn_readfirstlane((int)scr[8]);
      if (n <= 64) {
        const v4u mine = cand[l];
        int rank = 0;
        for (int i = 0; i < n; i += 16) {
          v4u kk[4];
#pragma unroll
          for (int e = 0; e < 4; ++e) kk[e] = *(const v4u LAS*)(ckey + i + 4 * e);
#pragma unroll
          for (int e = 0; e < 16; ++e) rank += kk[e >> 2][e & 3] > mine[0] ? 1 : 0;
        }
        if (l < n && rank < 8) picks[rank] = mine;
      } else {
        for (int c0 = 0; c0 < n; c0 += 64) {
          if (c0 + l < n) {
            const v4u mine = cand[c0 + l];
            int rank = 0;
            for (int i = 0; i < n; ++i) rank += ckey[i] > mine[0] ? 1 : 0;
            if (rank < 8) picks[rank] = mine;
          }
        }
      }
      asm volatile("s_waitcnt lgkmcnt(0)" ::: "memory");
      const v4u my = picks[l & 7];
      const int st = 0xfffe - (int)(my[0] & 0xffffu);
      STAMP(1 + r, 10);
      v4u xa[2], aa[2];
      const unsigned ro = (unsigned)(st * 1024 + 128 * w + 16 * g4);
      _Pragma("unroll 1") for (unsigned spins = 0; spins < SPIN; ++spins) {
#pragma unroll
        for (int i = 0; i < 2; ++i) {
          xa[i] = r == 0 ? *(const v4u*)((const char*)X + ro + 64 * i) : LD128(ro + 64 * i, OFF_X2);
          aa[i] = LD128(ro + 64 * i, OFF_AA + r * (2u << 20));
        }
        if (spins == 0 && gu_u == 0 && w == 0) {
          ST128(my, lane16, SM_IDX + r * 1024);
          if (r == 0 && l == 0)
            __builtin_amdgcn_raw_buffer_store_b64((v2u){ep_lo + 1u, ep_hi + (ep_lo == 0xffffffffu ? 1u : 0u)}, rws, 0, FL_E, 16);
        }
        if (__all(max(umax4(aa[0]), umax4(aa[1])) != SENT)) break;
        SLEEP(2);
      }
      STAMP(1 + r, 11);
      const float mb = __uint_as_float(my[1]), ri = __uint_as_float(my[2]);
      f32x4 ag = {0, 0, 0, 0}, au = {0, 0, 0, 0};
#pragma unroll
      for (int i = 0; i < 2; ++i) {
        v4u y;
#pragma unroll
        for (int e = 0; e < 4; ++e) y[e] = norm2(unp(pack2(unp(xa[i][e]) + unp(aa[i][e]))), mb, ri);
        ag = mfma(ffw[2 * i], y, ag);
        au = mfma(ffw[2 * i + 1], y, au);
      }
      f32x4 LAS* red = (f32x4 LAS*)(sm + L_SH + 20480);
      if (tok < 8) { red[w * 64 + g4 * 8 + tok] = ag; red[w * 64 + 32 + g4 * 8 + tok] = au; }
      BAR();
      if (w == 0) {
        const f32x2 LAS* rd = (const f32x2 LAS*)red + l;
        f32x2 gt = rd[0], up = rd[64];
#pragma unroll
        for (int i = 1; i < 8; ++i) { gt += rd[i * 128]; up += rd[i * 128 + 64]; }
        const f32x2 gv = unp(pack2(gt)), uv = unp(pack2(up));
        f32x2 sg;
#pragma unroll
        for (int e = 0; e < 2; ++e) sg[e] = gv[e] * __builtin_amdgcn_rcpf(1.0f + __expf(-gv[e]));
        const unsigned o = pack2(unp(pack2(sg)) * uv);
        __builtin_amdgcn_raw_buffer_store_b32(o, rws, gu_u * 256 + (l >> 5) * 128 + ((l >> 1) & 7) * 16 + ((l >> 4) & 1) * 8 + (l & 1) * 4,
                                              SM_HF + r * 32768, 16);
      }
      BAR();
    }
    STAMP(1 + r, 12);
    if (r == 0) qkv_gemm<1>(sm, rws, w0, tag_lo, tag_hi, Wr, Wg, Wu, Wd, wrv, wof, wv);
    STAMP(1 + r, 13);
    v4u srec;
    {
      IDS
      v4u LAS* scr = (v4u LAS*)(sm + L_SH + 28672);
      if (w == 7) {
        v4u f;
        _Pragma("unroll 1") for (unsigned spins = 0; spins < SPIN; ++spins) {
          f = LD128(xcd * 128 + (l & 7) * 16, SM_IDX + r * 1024);
          if (__all(valid4(f))) break;
          SLEEP(4);
        }
        if (l < 8) scr[l] = f;
      }
      BAR();
      srec = scr[l & 7];
    }
    const int stok = 0xfffe - (int)(srec[0] & 0xffffu);
    if (role == 2) {
      IDS
      if (r == 0) wait_all_flags(rws, l, FL_C, tag_lo + 3, tag_hi);
      v4u ffw[7];
#pragma unroll
      for (int i = 0; i < 7; ++i) ffw[i] = LD128(lane16, OFF_DNF + (dn_u * 56 + 7 * w + i) * 1024);
      v2u xa, aa;
      const unsigned ro = (unsigned)(stok * 1024 + dn_u * 32 + 8 * g4);
      _Pragma("unroll 1") for (unsigned spins = 0; spins < SPIN; ++spins) {
        xa = r == 0 ? *(const v2u*)((const char*)X + ro) : __builtin_amdgcn_raw_buffer_load_b64(rws, ro, OFF_X2, 16);
        aa = __builtin_amdgcn_raw_buffer_load_b64(rws, ro, OFF_AA + r * (2u << 20), 16);
        if (__all(max(aa[0], aa[1]) != SENT)) break;
        SLEEP(2);
      }
      v4u hf[7];
      _Pragma("unroll 1") for (unsigned spins = 0; spins < SPIN; ++spins) {
        unsigned mx = 0;
#pragma unroll
        for (int s = 0; s < 7; ++s) {
          hf[s] = LD128((7 * w + s) * 512 + g4 * 128 + (l & 7) * 16, SM_HF + r * 32768);
          mx = max(mx, umax4(hf[s]));
        }
        if (__all(mx != SENT)) break;
        SLEEP(3);
      }
      STAMP(1 + r, 14);
      f32x4 acc = {0, 0, 0, 0};
#pragma unroll
      for (int s = 0; s < 7; ++s) acc = mfma(ffw[s], hf[s], acc);
      f32x4 LAS* red = (f32x4 LAS*)(sm + L_OS + 16384);
      red[w * 64 + l] = acc;
      BAR();
      if (w == 0 && tok < 8) {
        f32x4 y = red[l];
#pragma unroll
        for (int i = 1; i < 8; ++i) y += red[i * 64 + l];
        unsigned u = srec[0] >> 16;
        u = (u & 0x8000u) ? (u ^ 0x8000u) : (u ^ 0xffffu);
        const float rr = __uint_as_float(u << 16);
        const float gt = rbf(__builtin_amdgcn_rcpf(1.0f + __expf(-rr)));
        const float mb = __uint_as_float(srec[1]), ri = __uint_as_float(srec[2]);
        v2u o;
#pragma unroll
        for (int e = 0; e < 2; ++e) {
          const f32x2 x1 = unp(pack2(unp(xa[e]) + unp(aa[e])));
          const f32x2 y1 = unp(norm2(x1, mb, ri));
          const f32x2 up = unp(pack2(unp(pack2(y[2 * e], y[2 * e + 1])) * gt));
          o[e] = pack2(x1 + unp(pack2(y1 + up)));
        }
        if (r == 0) __builtin_amdgcn_raw_buffer_store_b64(o, rws, tok * 1024 + dn_u * 32 + 8 * g4, SM_X2S, 16);
        else *(v2u*)((char*)Out + ro) = o;
      }
      BAR();
    }
    {
      IDS
      const unsigned long long own = __ballot(stok == myrow);
      if (r == 0) {
        const int lo = 128 * t - 16;
        const bool inr = stok >= lo && stok < lo + 144;
        const unsigned inmask = (unsigned)__ballot(inr) & 0xffu;
        if (inmask) {
          if ((inmask >> w) & 1) {
            v4u xv;
            _Pragma("unroll 1") for (unsigned spins = 0; spins < SPIN; ++spins) {
              xv = LD128(lane16, SM_X2S + w * 1024);
              if (__all(valid4(xv))) break;
              SLEEP(3);
            }
            f32x2 x[4] = {unp(xv[0]), unp(xv[1]), unp(xv[2]), unp(xv[3])};
            float mb, ri;
            *(v4u LAS*)(sm + L_OS + l * 128 + w * 16) = ln_row(x, mb, ri);
          }
          BAR();
          if (w < 6) {
            f32x4 acc = {0, 0, 0, 0};
#pragma unroll
            for (int s = 0; s < 16; ++s)
              acc = mfma(*(const v4u LAS*)(sm + L_W + (w >> 1) * 32768 + (w & 1) * 1024 + s * 2048 + lane16),
                         *(const v4u LAS*)(sm + L_OS + (4 * s + g4) * 128 + (l & 7) * 16), acc);
            if (inr && tok < 8)
              *(v2u LAS*)(sm + L_SH + (2 * w + (g4 >> 1)) * PLANE + 8 * (g4 & 1) + (stok - lo) * 16) = packacc(acc);
          }
          if (own) {
            const int slot = __builtin_ctzll(own);
            _Pragma("unroll 1") for (unsigned spins = 0; spins < SPIN; ++spins) {
              xres = LD128(lane16, SM_X2S + slot * 1024);
              if (__all(valid4(xres))) break;
              SLEEP(2);
            }
            ST128(xres, lane16, OFF_X2 + myrow * 1024);
          }
          BAR();
        }
        STAMP(2, 2);
        attention(sm, rws, w0, 1);
      } else if (!own) {
        *(v4u*)((char*)Out + myrow * 1024 + lane16) = xo;
      }
    }
    STAMP(1 + r, 15);
  }
}

extern "C" void kernel_launch(void* const* d_in, const int* in_sizes, int n_in, void* d_out, int out_size,
                              void* d_ws, size_t ws_size, hipStream_t stream) {
  (void)hipFuncSetAttribute((const void*)k_main, hipFuncAttributeMaxDynamicSharedMemorySize, L_TOTAL);
  k_main<<<256, 512, L_TOTAL, stream>>>((const u16*)d_in[0], (const u16*)d_in[1], (const u16*)d_in[2], (const u16*)d_in[4],
                                        (const u16*)d_in[5], (const u16*)d_in[6], (const u16*)d_in[7], (u16*)d_out, (char*)d_ws);
}
```

```cpp
#include <hip/hip_runtime.h>
#include <hip/hip_bf16.h>
#include <stdint.h>

#define LAS __attribute__((address_space(3)))
typedef unsigned short u16;
typedef short v4s __attribute__((ext_vector_type(4)));
typedef short v8s __attribute__((ext_vector_type(8)));
typedef __bf16 v2bf __attribute__((ext_vector_type(2)));
typedef float f32x4 __attribute__((ext_vector_type(4)));
typedef float f32x2 __attribute__((ext_vector_type(2)));
typedef unsigned v4u __attribute__((ext_vector_type(4)));
typedef unsigned v2u __attribute__((ext_vector_type(2)));

#define STAMP(k, i)
#define TS(v)
#define TSPUT(k, i, v)

constexpr int S = 2048, D = 512, F = 1792;
constexpr unsigned SENT = 0xFFFFFFFFu;
constexpr unsigned SPIN = 1u << 18;
constexpr unsigned OFF_Z1 = 0, OFF_Z2 = 2u << 20, OFF_OF = 4u << 20, OFF_AA = 8u << 20, OFF_X2 = 12u << 20,
                   OFF_SM = 14u << 20, OFF_WF = 16u << 20, OFF_WOF = 18u << 20, OFF_GUF = 19u << 20,
                   OFF_DNF = 23u << 20, WS_BYTES = 25u << 20;
constexpr unsigned SM_REC = OFF_SM, SM_HF = OFF_SM + 65536, SM_X2S = OFF_SM + 131072, SM_IDX = OFF_SM + 139264;
constexpr unsigned FL_A = OFF_SM + 262144;
constexpr unsigned FL_B = FL_A + 65536;
constexpr unsigned FL_C = FL_B + 4096;
constexpr unsigned FL_E = FL_C + 4096;
constexpr int L_W = 0, L_SH = 98304, L_OS = 131072, L_TOTAL = 163840;
constexpr int PLANE = 2304;

__device__ __forceinline__ float bf_lo(unsigned u) { return __uint_as_float(u << 16); }
__device__ __forceinline__ float bf_hi(unsigned u) { return __uint_as_float(u & 0xffff0000u); }
__device__ __forceinline__ unsigned pack2(f32x2 f) {
  v2bf c = __builtin_convertvector(f, v2bf);
  return __builtin_bit_cast(unsigned, c);
}
__device__ __forceinline__ unsigned pack2(float a, float b) { return pack2((f32x2){a, b}); }
__device__ __forceinline__ f32x2 unp(unsigned u) { return (f32x2){bf_lo(u), bf_hi(u)}; }
__device__ __forceinline__ float rbf(float a) {
  __bf16 c = (__bf16)a;
  return __uint_as_float(((unsigned)__builtin_bit_cast(u16, c)) << 16);
}
__device__ __forceinline__ float dot2(unsigned a, unsigned b, float c) {
  return __builtin_amdgcn_fdot2_f32_bf16(__builtin_bit_cast(v2bf, a), __builtin_bit_cast(v2bf, b), c, false);
}
__device__ __forceinline__ f32x4 mfma(v4u a, v4u b, f32x4 c) {
  return __builtin_amdgcn_mfma_f32_16x16x32_bf16(__builtin_bit_cast(v8s, a), __builtin_bit_cast(v8s, b), c, 0, 0, 0);
}
__device__ __forceinline__ v2u packacc(f32x4 a) { return (v2u){pack2(a[0], a[1]), pack2(a[2], a[3])}; }
typedef __amdgpu_buffer_rsrc_t srd_t;
__device__ __forceinline__ srd_t make_srd(const void* p, unsigned bytes) {
  return __builtin_amdgcn_make_buffer_rsrc((void*)p, 0, bytes, 0x00020000);
}
__device__ __forceinline__ void dma16(srd_t srd, unsigned voff, unsigned soff, unsigned lds) {
  unsigned keep;
  asm volatile("s_mov_b32 %0, m0\n\ts_mov_b32 m0, %3\n\ts_nop 0\n\tbuffer_load_dwordx4 %1, %2, %4 offen lds\n\ts_mov_b32 m0, %0"
               : "=&s"(keep) : "v"(voff), "s"(srd), "s"(lds), "s"(soff));
}
__device__ __forceinline__ void dma16_sc1(srd_t srd, unsigned voff, unsigned soff, unsigned lds) {
  unsigned keep;
  asm volatile("s_mov_b32 %0, m0\n\ts_mov_b32 m0, %3\n\ts_nop 0\n\tbuffer_load_dwordx4 %1, %2, %4 offen sc1 lds\n\ts_mov_b32 m0, %0"
               : "=&s"(keep) : "v"(voff), "s"(srd), "s"(lds), "s"(soff));
}
__device__ __forceinline__ void aload16(v4u& d, srd_t srd, unsigned voff, unsigned soff) {
  asm volatile("buffer_load_dwordx4 %0, %1, %2, %3 offen" : "=v"(d) : "v"(voff), "s"(srd), "s"(soff));
}
__device__ __forceinline__ void aload16_sc1(v4u& d, srd_t srd, unsigned voff, unsigned soff) {
  asm volatile("buffer_load_dwordx4 %0, %1, %2, %3 offen sc1" : "=v"(d) : "v"(voff), "s"(srd), "s"(soff));
}
__device__ __forceinline__ void astore16_sc1(v4u d, srd_t srd, unsigned voff, unsigned soff) {
  asm volatile("buffer_store_dwordx4 %0, %1, %2, %3 offen sc1\n\ts_nop 1" :: "v"(d), "v"(voff), "s"(srd), "s"(soff));
}
#define SLEEP(n) do { __builtin_amdgcn_s_sleep(n); asm volatile("" ::: "memory"); } while (0)
#define VMWAIT(n) asm volatile("s_waitcnt vmcnt(" #n ")" ::: "memory")
#define BAR() do { asm volatile("s_waitcnt lgkmcnt(0)" ::: "memory"); __builtin_amdgcn_s_barrier(); asm volatile("" ::: "memory"); } while (0)
#define LD128(voff, soff) __builtin_amdgcn_raw_buffer_load_b128(rws, voff, soff, 16)
__device__ __forceinline__ void st128(v4u d, srd_t srd, unsigned voff, unsigned soff) {
  asm volatile("buffer_store_dwordx4 %0, %1, %2, %3 offen sc1\n\ts_nop 1" :: "v"(d), "v"(voff), "s"(srd), "s"(soff) : "memory");
}
#define ST128(v, voff, soff) st128(v, rws, voff, soff)
__device__ __forceinline__ unsigned umax4(v4u v) { return max(max(v[0], v[1]), max(v[2], v[3])); }
__device__ __forceinline__ bool valid4(v4u v) { return umax4(v) != SENT; }

template <int CTRL, int RMASK>
__device__ __forceinline__ float dpp_add(float v) {
  int t = __builtin_amdgcn_update_dpp(0, __float_as_int(v), CTRL, RMASK, 0xf, true);
  return v + __int_as_float(t);
}
__device__ __forceinline__ float wave_sum(float v) {
  v = dpp_add<0x111, 0xf>(v); v = dpp_add<0x112, 0xf>(v); v = dpp_add<0x114, 0xf>(v); v = dpp_add<0x118, 0xf>(v);
  v = dpp_add<0x142, 0xa>(v); v = dpp_add<0x143, 0xc>(v);
  return __int_as_float(__builtin_amdgcn_readlane(__float_as_int(v), 63));
}
template <int CTRL, int RMASK>
__device__ __forceinline__ unsigned dpp_max(unsigned v) {
  unsigned t = (unsigned)__builtin_amdgcn_update_dpp(0, (int)v, CTRL, RMASK, 0xf, true);
  return v > t ? v : t;
}
__device__ __forceinline__ unsigned wave_max(unsigned v) {
  v = dpp_max<0x111, 0xf>(v); v = dpp_max<0x112, 0xf>(v); v = dpp_max<0x114, 0xf>(v); v = dpp_max<0x118, 0xf>(v);
  v = dpp_max<0x142, 0xa>(v); v = dpp_max<0x143, 0xc>(v);
  return (unsigned)__builtin_amdgcn_readlane((int)v, 63);
}
__device__ __forceinline__ unsigned norm2(f32x2 x, float mb, float ri) { return pack2(unp(pack2(x - mb)) * ri); }
__device__ __forceinline__ v4u ln_row(const f32x2 (&x)[4], float& mb, float& ri) {
  const float c = __int_as_float(__builtin_amdgcn_readfirstlane(__float_as_int(x[0][0])));
  const f32x2 d0 = x[0] - c, d1 = x[1] - c, d2 = x[2] - c, d3 = x[3] - c;
  const f32x2 s2 = (d0 + d1) + (d2 + d3);
  const f32x2 q2 = (d0 * d0 + d1 * d1) + (d2 * d2 + d3 * d3);
  float s = s2[0] + s2[1], q = q2[0] + q2[1];
  s = dpp_add<0x111, 0xf>(s); q = dpp_add<0x111, 0xf>(q);
  s = dpp_add<0x112, 0xf>(s); q = dpp_add<0x112, 0xf>(q);
  s = dpp_add<0x114, 0xf>(s); q = dpp_add<0x114, 0xf>(q);
  s = dpp_add<0x118, 0xf>(s); q = dpp_add<0x118, 0xf>(q);
  s = dpp_add<0x142, 0xa>(s); q = dpp_add<0x142, 0xa>(q);
  s = dpp_add<0x143, 0xc>(s); q = dpp_add<0x143, 0xc>(q);
  const float dm = __int_as_float(__builtin_amdgcn_readlane(__float_as_int(s), 63)) * (1.0f / D);
  const float var = fmaxf(__int_as_float(__builtin_amdgcn_readlane(__float_as_int(q), 63)) * (1.0f / D) - dm * dm, 0.f);
  mb = rbf(c + dm);
  const float sg = rbf(__builtin_amdgcn_sqrtf(rbf(rbf(var) + 1e-5f)));
  ri = __builtin_amdgcn_rcpf(sg);
  return (v4u){norm2(x[0], mb, ri), norm2(x[1], mb, ri), norm2(x[2], mb, ri), norm2(x[3], mb, ri)};
}

#define IDS                                                                                              \
  int tid = threadIdx.x, w = w0, b = blockIdx.x;                                                         \
  asm volatile("" : "+v"(tid)); asm volatile("" : "+s"(w)); asm volatile("" : "+s"(b));                  \
  const int l = tid & 63, xcd = b & 7, jj = b >> 3;                                                      \
  const int t = 4 * (xcd >> 1) + (jj >> 3), j = 8 * (xcd & 1) + (jj & 7);                                \
  const int g4 = l >> 4, tok = l & 15;                                                                   \
  const int rq = j >> 2, cq = j & 3, myrow = 128 * t + 8 * j + w;                                        \
  const int gu_u = 14 * xcd + jj, dn_u = 4 * xcd + (jj - 14);                                            \
  const unsigned lane16 = l * 16;                                                                        \
  const int rth = t > 0 ? 8 * t - 1 : 0;                                                                 \
  (void)rq; (void)cq; (void)myrow; (void)gu_u; (void)dn_u; (void)lane16; (void)rth; (void)tok; (void)g4;

__device__ __forceinline__ void attention(char LAS* sm, srd_t rws, int w0, int r) {
  IDS
  const char LAS* rp = sm + L_SH + g4 * PLANE + (16 + 16 * w + tok) * 16;
  const v4u q0 = *(const v4u LAS*)rp;
  float d[8];
#pragma unroll
  for (int k = 0; k < 8; ++k) {
    const v4u k0 = *(const v4u LAS*)(rp + 4 * PLANE - 32 * k);
    d[k] = dot2(q0[3], k0[3], dot2(q0[2], k0[2], dot2(q0[1], k0[1], dot2(q0[0], k0[0], 0.f))));
  }
  float lg[4];
#pragma unroll
  for (int k = 0; k < 4; ++k) {
    const auto sw = __builtin_amdgcn_permlane16_swap(__float_as_uint(d[k]), __float_as_uint(d[k + 4]), false, false);
    lg[k] = rbf(__uint_as_float(sw[0]) + __uint_as_float(sw[1]));
  }
  if (t == 0) {
#pragma unroll
    for (int k = 0; k < 4; ++k) lg[k] = (16 * w + tok - 2 * k - 8 * (g4 & 1) >= 0) ? lg[k] : -4e9f;
  }
  float m = fmaxf(fmaxf(lg[0], lg[1]), fmaxf(lg[2], lg[3]));
  {
    const auto sw = __builtin_amdgcn_permlane16_swap(__float_as_uint(m), __float_as_uint(m), false, false);
    m = fmaxf(__uint_as_float(sw[0]), __uint_as_float(sw[1]));
  }
  constexpr float C = 0.25f * 1.44269504088896341f;
#pragma unroll
  for (int k = 0; k < 4; ++k) lg[k] = __builtin_amdgcn_exp2f((lg[k] - m) * C);
  float sum = (lg[0] + lg[1]) + (lg[2] + lg[3]);
  {
    const auto sw = __builtin_amdgcn_permlane16_swap(__float_as_uint(sum), __float_as_uint(sum), false, false);
    sum = __uint_as_float(sw[0]) + __uint_as_float(sw[1]);
  }
  const float inv = __builtin_amdgcn_rcpf(sum);
  const unsigned pa = pack2(lg[0] * inv, lg[1] * inv), pb = pack2(lg[2] * inv, lg[3] * inv);
  const auto sa = __builtin_amdgcn_permlane16_swap(pa, pa, false, false);
  const auto sb = __builtin_amdgcn_permlane16_swap(pb, pb, false, false);
  const unsigned pp[4] = {sa[0], sb[0], sa[1], sb[1]};
  float o[8];
#pragma unroll
  for (int e = 0; e < 8; ++e) o[e] = 0.f;
#pragma unroll
  for (int k = 0; k < 4; ++k) {
    const v4u v0 = *(const v4u LAS*)(rp + 8 * PLANE - 64 * k), v1 = *(const v4u LAS*)(rp + 8 * PLANE - 64 * k - 32);
#pragma unroll
    for (int e = 0; e < 4; ++e) {
      o[2 * e] = dot2(pp[k], __builtin_amdgcn_perm(v1[e], v0[e], 0x05040100u), o[2 * e]);
      o[2 * e + 1] = dot2(pp[k], __builtin_amdgcn_perm(v1[e], v0[e], 0x07060302u), o[2 * e + 1]);
    }
  }
  const v4u ov = {pack2(o[0], o[1]), pack2(o[2], o[3]), pack2(o[4], o[5]), pack2(o[6], o[7])};
  ST128(ov, lane16, OFF_OF + r * (2u << 20) + ((8 * t + w) * 16 + j) * 1024);
}

struct Unit { srd_t srd; unsigned stride, dst, dstep, dodd; };
__device__ __forceinline__ Unit ffn_unit(int u, const u16* Wg, const u16* Wu, const u16* Wd) {
  Unit r;
  const char* src = (const char*)Wd;
  r.stride = 1024; r.dst = WS_BYTES; r.dstep = 0; r.dodd = 0;
  if (u < 896) {
    const int up = u >= 448, v = u - 448 * up, s = v / 28, nb = v % 28;
    src = (const char*)(up ? Wu : Wg) + (size_t)(32 * s) * (F * 2) + nb * 128; r.stride = F * 2;
    r.dst = OFF_GUF + ((4 * nb * 16 + s) * 2 + up) * 1024; r.dstep = 2 * 32 * 1024; r.dodd = 32 * 1024;
  } else if (u < 1344) {
    const int s = (u - 896) >> 3, nb = u & 7;
    src = (const char*)Wd + (size_t)(32 * s) * 1024 + nb * 128;
    r.dst = OFF_DNF + (4 * nb * 56 + s) * 1024; r.dstep = 2 * 56 * 1024; r.dodd = 56 * 1024;
  }
  r.srd = make_srd(src, u < 1344 ? 32 * F * 2 : 0);
  return r;
}
__device__ __forceinline__ void unit_store(char LAS* scr, srd_t rws, int l, const v4u (&wv)[4], unsigned dst, unsigned dstep, unsigned dodd) {
#pragma unroll
  for (int i = 0; i < 4; ++i) *(v4u LAS*)(scr + i * 1024 + l * 16) = wv[i];
  asm volatile("s_waitcnt lgkmcnt(0)" ::: "memory");
  const char LAS* p = scr + (8 * (l >> 4) + ((l & 15) >> 2)) * 128 + 8 * (l & 3);
  v4s a[4], c[4];
#pragma unroll
  for (int i = 0; i < 4; ++i) {
    a[i] = __builtin_amdgcn_ds_read_tr16_b64_v4i16((LAS v4s*)(p + 32 * i));
    c[i] = __builtin_amdgcn_ds_read_tr16_b64_v4i16((LAS v4s*)(p + 32 * i + 512));
  }
#pragma unroll
  for (int i = 0; i < 4; ++i) {
    const v8s f = {a[i][0], a[i][1], a[i][2], a[i][3], c[i][0], c[i][1], c[i][2], c[i][3]};
    astore16_sc1(__builtin_bit_cast(v4u, f), rws, l * 16, dst + (i >> 1) * dstep + (i & 1) * dodd);
  }
  asm volatile("" ::: "memory");
}
__device__ __forceinline__ void wait_all_flags(srd_t rws, int l, unsigned off, unsigned lo, unsigned hi) {
  _Pragma("unroll 1") for (unsigned spins = 0; spins < SPIN; ++spins) {
    const v4u a = LD128(l * 32, off), c = LD128(l * 32 + 16, off);
    if (__all((((a[0] ^ lo) | (a[1] ^ hi)) | ((a[2] ^ lo) | (a[3] ^ hi)) | ((c[0] ^ lo) | (c[1] ^ hi)) | ((c[2] ^ lo) | (c[3] ^ hi))) == 0u)) break;
    SLEEP(3);
  }
}
constexpr int L_CTL = L_SH + 32704;
#define WFRAG(s, ct) ((6 * (s) + (ct)) * 1024)

#define WAIT_TIE4(n, a, i) do { VMWAIT(n); __builtin_amdgcn_sched_barrier(0); asm volatile("" : "+v"(a[i]), "+v"(a[i + 1]), "+v"(a[i + 2]), "+v"(a[i + 3])); } while (0)
template <int R>
__device__ __forceinline__ void qkv_gemm(char LAS* sm, srd_t rws, int w0, unsigned tag_lo, unsigned tag_hi,
                                         const u16* Wr, const u16* Wg, const u16* Wu, const u16* Wd, v4u& wrv,
                                         v4u (&wof)[16], v4u (&fv)[4]) {
  IDS
  if (w < (R ? 1 : 2)) {
    int fi = l < 16 ? 16 * t + l : (t > 0 ? 16 * t - 2 + (l & 1) : 0);
    if (R == 0 && l >= 32) fi = 16 * ((l >> 1) & 15) + (j & 14) + (l & 1);
    if (R == 0 && w == 1) fi = 16 * (l & 15) + 4 * cq + 2 * ((l >> 4) & 1) + 1;
    _Pragma("unroll 1") for (unsigned spins = 0; spins < SPIN; ++spins) {
      const v2u f = __builtin_amdgcn_raw_buffer_load_b64(rws, fi * 128, FL_A + R * 32768, 16);
      if (__all(((f[0] ^ (tag_lo + R)) | (f[1] ^ tag_hi)) == 0u)) break;
      SLEEP(3);
    }
  }
  BAR();
  STAMP(3, 4 + 4 * R);
  v4u z[16], zh[2];
  const unsigned zo = R ? OFF_Z2 : OFF_Z1, zb = zo + (8 * t + w) * 16384;
  const unsigned wb = OFF_WF + j * 98304 + 3 * w * 1024;
  aload16_sc1(zh[0], rws, lane16, zo + (rth * 16 + 2 * w) * 1024);
  aload16_sc1(zh[1], rws, lane16, zo + (rth * 16 + 2 * w + 1) * 1024);
#pragma unroll
  for (int c = 0; c < 2; ++c) {
    if (R == 0) {
#pragma unroll
      for (int p = 0; p < 3; ++p) dma16_sc1(rws, lane16, wb + (24 * c + p) * 1024, L_W + (24 * c + 3 * w + p) * 1024);
    }
#pragma unroll
    for (int s = 0; s < 4; ++s) aload16_sc1(z[4 * c + s], rws, lane16, zb + (4 * c + s) * 1024);
    if (R == 0 && c == 0) {
      VMWAIT(9);
      __builtin_amdgcn_sched_barrier(0);
      asm volatile("" : "+v"(fv[0]), "+v"(fv[1]), "+v"(fv[2]), "+v"(fv[3]));
      const Unit un = ffn_unit(w >= 2 ? (w - 2) * 256 + b : 1344, Wg, Wu, Wd);
      unit_store(sm + L_OS + w * 4096, rws, l, fv, un.dst, un.dstep, un.dodd);
    }
  }
  const char LAS* wp = sm + L_W + lane16;
  const char LAS* wh = wp + (6 * (w >> 2) + 2 + (w & 3)) * 1024;
  const char LAS* hp = sm + L_SH + lane16 + (w >> 2) * 1024;
  v4u hw, hz;
  v4u f[2][6];
  unsigned long long tq[12];
  TS(tq[0]);
  f32x4 acc[6], acch = {0, 0, 0, 0};
#pragma unroll
  for (int c = 0; c < 6; ++c) acc[c] = (f32x4){0, 0, 0, 0};
  const unsigned wob = OFF_WOF + (8 * cq + w) * 16384;
#pragma unroll
  for (int c = 0; c < 4; ++c) {
    if (R == 0) {
      if (c == 0) WAIT_TIE4(11, z, 0); else if (c < 3) WAIT_TIE4(7, z, 4 * c); else WAIT_TIE4(8, z, 12);
    } else {
      if (c < 3) WAIT_TIE4(4, z, 4 * c); else WAIT_TIE4(0, z, 12);
    }
    if (c == 0) {
      asm volatile("" : "+v"(zh[0]), "+v"(zh[1]));
      *(v4u LAS*)(sm + L_SH + (2 * w) * 1024 + lane16) = zh[0];
      *(v4u LAS*)(sm + L_SH + (2 * w + 1) * 1024 + lane16) = zh[1];
    }
    TS(tq[1 + 2 * c]);
    if (R == 0 || c == 0) {
      BAR();
      if (R == 0 && c == 1 && tid == 448) __builtin_amdgcn_raw_buffer_store_b64((v2u){tag_lo + 2, tag_hi}, rws, b * 8, FL_B, 16);
#pragma unroll
      for (int ct = 0; ct < 6; ++ct) f[0][ct] = *(const v4u LAS*)(wp + WFRAG(4 * c, ct));
    }
    TS(tq[2 + 2 * c]);
#pragma unroll
    for (int s4 = 0; s4 < 4; ++s4) {
      const int s = 4 * c + s4;
      if (R ? s < 15 : s4 < 3) {
#pragma unroll
        for (int ct = 0; ct < 6; ++ct) f[(s + 1) & 1][ct] = *(const v4u LAS*)(wp + WFRAG(s + 1, ct));
      }
      if (!(s & 1)) { hw = *(const v4u LAS*)(wh + s * 6144); hz = *(const v4u LAS*)(hp + s * 1024); }
      __builtin_amdgcn_sched_barrier(0);
#pragma unroll
      for (int ct = 0; ct < 6; ++ct) acc[ct] = mfma(f[s & 1][ct], z[s], acc[ct]);
      if (s & 1) acch = mfma(hw, hz, acch);
      if (c < 2) {
        if (R == 0 && s4 < 3) dma16_sc1(rws, lane16, wb + (24 * (c + 2) + s4) * 1024, L_W + (24 * (c + 2) + 3 * w + s4) * 1024);
        aload16_sc1(z[4 * (c + 2) + s4], rws, lane16, zb + (4 * (c + 2) + s4) * 1024);
      } else if (R == 0) {
        aload16_sc1(wof[8 * (c - 2) + 2 * s4], rws, lane16, wob + (8 * (c - 2) + 2 * s4) * 1024);
        aload16_sc1(wof[8 * (c - 2) + 2 * s4 + 1], rws, lane16, wob + (8 * (c - 2) + 2 * s4 + 1) * 1024);
        if (s == 15) aload16(wrv, make_srd(Wr, D * 2), lane16, 0);
      }
      __builtin_amdgcn_sched_barrier(0);
    }
  }
  TS(tq[9]);
#pragma unroll
  for (int i = 0; i < 10; ++i) TSPUT(4 + R, i, tq[i]);
  f32x4 LAS* hx = (f32x4 LAS*)(sm + L_SH + 27648 + (w & 3) * 1024 + lane16);
  if (w >= 4) *hx = acch;
  BAR();
  char LAS* qp = sm + L_SH + (g4 >> 1) * PLANE + 8 * (g4 & 1);
#pragma unroll
  for (int c = 0; c < 6; ++c) *(v2u LAS*)(qp + 2 * c * PLANE + (16 + 16 * w + tok) * 16) = packacc(acc[c]);
  if (w < 4) *(v2u LAS*)(qp + 2 * (2 + w) * PLANE + tok * 16) = packacc(acch + *hx);
}

__global__ __launch_bounds__(512, 2) void k_main(const u16* __restrict__ X, const u16* __restrict__ Wqkv,
                                                 const u16* __restrict__ Wo, const u16* __restrict__ Wr,
                                                 const u16* __restrict__ Wg, const u16* __restrict__ Wu,
                                                 const u16* __restrict__ Wd, u16* __restrict__ Out,
                                                 char* __restrict__ ws) {
  extern __shared__ __attribute__((aligned(16))) char smem[];
  char LAS* sm = (char LAS*)smem;
  asm volatile("" :: "s"(X), "s"(Wqkv), "s"(Wo), "s"(Wr), "s"(Wg), "s"(Wu), "s"(Wd), "s"(Out), "s"(ws));
  const int w0 = __builtin_amdgcn_readfirstlane(threadIdx.x >> 6);
  const srd_t rws = make_srd(ws, WS_BYTES);
  const int role = (blockIdx.x >> 3) < 14 ? 1 : (blockIdx.x >> 3) < 18 ? 2 : 0;
  STAMP(1, 0);
  v4u xres, wrv;
  v4u wof[16];
  unsigned tag_lo, tag_hi, ep_lo, ep_hi;
  v4u fv[4];
  {
    IDS
    v2u ev;
    v4u wv[4];
    asm volatile("buffer_load_dwordx2 %0, off, %1, %2 sc1" : "=v"(ev) : "s"(rws), "s"(FL_E));
    aload16(xres, make_srd(X, S * D * 2), lane16, myrow * 1024);
    const bool wo_unit = w == 1 && (j & 1);
    const int up = w == 0 ? (j & 1) : 2;
    const unsigned ustride = wo_unit ? 1024 : 3072;
    {
      const srd_t su = make_srd(wo_unit ? (const char*)Wo + (size_t)(32 * t) * 1024 + (j >> 1) * 128
                                        : (const char*)Wqkv + (size_t)(32 * t) * 3072 + up * 1024 + (j >> 1) * 128,
                                w < 2 ? 31 * ustride + 128 : 0);
#pragma unroll
      for (int i = 0; i < 4; ++i) aload16(wv[i], su, (unsigned)(l >> 3) * ustride + (l & 7) * 16, 8 * i * ustride);
    }
    VMWAIT(4);
    __builtin_amdgcn_sched_barrier(0);
    asm volatile("" : "+v"(ev), "+v"(xres));
    STAMP(3, 0);
    ep_lo = __builtin_amdgcn_readfirstlane(ev[0]);
    ep_hi = __builtin_amdgcn_readfirstlane(ev[1]);
    {
      const unsigned long long e = ((unsigned long long)ep_hi << 32) | ep_lo;
      const unsigned long long tg = (e + 1) * 0x9E3779B97F4A7C15ull;
      tag_lo = (unsigned)tg & ~3u; tag_hi = (unsigned)(tg >> 32);
    }
    {
      f32x2 x[4] = {unp(xres[0]), unp(xres[1]), unp(xres[2]), unp(xres[3])};
      float mb, ri;
      *(v4u LAS*)(sm + L_SH + 8192 + w * 1040 + lane16) = ln_row(x, mb, ri);
    }
    VMWAIT(0);
    __builtin_amdgcn_sched_barrier(0);
    asm volatile("" : "+v"(wv[0]), "+v"(wv[1]), "+v"(wv[2]), "+v"(wv[3]));
    BAR();
    if (w < 2) {
      if (wo_unit) unit_store(sm + L_SH + w * 4096, rws, l, wv, OFF_WOF + (4 * (j >> 1) * 16 + t) * 1024, 32768, 16384);
      else unit_store(sm + L_SH + w * 4096, rws, l, wv, OFF_WF + (((j & 14) * 16 + t) * 6 + 2 * up) * 1024, 98304, 1024);
    } else if (w >= 4) {
#pragma unroll
      for (int i = 0; i < 2; ++i) {
        const int chunk = 16 * (w - 4) + 8 * i + (l >> 3), r8 = l & 7;
        const v4u v = *(const v4u LAS*)(sm + L_SH + 8192 + r8 * 1040 + chunk * 16);
        ST128(v, (((8 * t + (j >> 1)) * 16 + (chunk >> 2)) * 64 + (chunk & 3) * 16 + 8 * (j & 1) + r8) * 16, OFF_Z1);
      }
    }
    VMWAIT(0);
    STAMP(3, 2);
    BAR();
    if (tid == 0) __builtin_amdgcn_raw_buffer_store_b64((v2u){tag_lo, tag_hi}, rws, (16 * t + j) * 128, FL_A, 16);
    {
      const Unit un = ffn_unit(w >= 2 ? (w - 2) * 256 + b : 1344, Wg, Wu, Wd);
#pragma unroll
      for (int i = 0; i < 4; ++i) aload16(fv[i], un.srd, (unsigned)(l >> 3) * un.stride + (l & 7) * 16, 8 * i * un.stride);
    }
    const v4u sv = {SENT, SENT, SENT, SENT};
    astore16_sc1(sv, rws, lane16, OFF_OF + ((8 * t + w) * 16 + j) * 1024);
    astore16_sc1(sv, rws, (unsigned)((128 * t + 32 * rq + 4 * w + g4) * 1024 + cq * 256 + tok * 16), OFF_AA);
    astore16_sc1(sv, rws, l < 36 ? lane16 : WS_BYTES, w == 2 ? OFF_SM + b * 576 : WS_BYTES);
    STAMP(3, 3);
  }
  qkv_gemm<0>(sm, rws, w0, tag_lo, tag_hi, Wr, Wg, Wu, Wd, wrv, wof, fv);
  BAR();
  STAMP(1, 2);
  attention(sm, rws, w0, 0);
  VMWAIT(1);
  __builtin_amdgcn_sched_barrier(0);
  asm volatile("" : "+v"(wrv), "+v"(wof[0]), "+v"(wof[1]), "+v"(wof[2]), "+v"(wof[3]), "+v"(wof[4]), "+v"(wof[5]),
               "+v"(wof[6]), "+v"(wof[7]), "+v"(wof[8]), "+v"(wof[9]), "+v"(wof[10]), "+v"(wof[11]), "+v"(wof[12]),
               "+v"(wof[13]), "+v"(wof[14]), "+v"(wof[15]));

#pragma unroll 1
  for (int r = 0; r < 2; ++r) {
    STAMP(1 + r, 3);
    {
      IDS
      v4u of[4];
      const unsigned ofb = OFF_OF + r * (2u << 20) + ((8 * t + 2 * rq) * 16 + 4 * w) * 1024;
      if (r == 0) wait_all_flags(rws, l, FL_B, tag_lo + 2, tag_hi);
      else SLEEP(10);
      _Pragma("unroll 1") for (unsigned spins = 0; spins < SPIN; ++spins) {
#pragma unroll
        for (int i = 0; i < 4; ++i) of[i] = LD128(lane16, ofb + i * 1024);
        if (__all(max(max(umax4(of[0]), umax4(of[1])), max(umax4(of[2]), umax4(of[3]))) != SENT)) break;
        SLEEP(2);
      }
#pragma unroll
      for (int i = 0; i < 4; ++i) *(v4u LAS*)(sm + L_OS + (4 * w + i) * 1024 + lane16) = of[i];
    }
    BAR();
    STAMP(1 + r, 4);
    {
      IDS
      f32x4 a0 = {0, 0, 0, 0}, a1 = {0, 0, 0, 0};
      const char LAS* op = sm + L_OS + lane16;
      v4u f[2][8];
#pragma unroll
      for (int i = 0; i < 4; ++i) { f[0][2 * i] = *(const v4u LAS*)(op + i * 1024); f[0][2 * i + 1] = *(const v4u LAS*)(op + (16 + i) * 1024); }
#pragma unroll
      for (int c = 0; c < 4; ++c) {
        if (c < 3) {
#pragma unroll
          for (int i = 0; i < 4; ++i) {
            f[(c + 1) & 1][2 * i] = *(const v4u LAS*)(op + (4 * c + 4 + i) * 1024);
            f[(c + 1) & 1][2 * i + 1] = *(const v4u LAS*)(op + (20 + 4 * c + i) * 1024);
          }
        }
        __builtin_amdgcn_sched_barrier(0);
#pragma unroll
        for (int i = 0; i < 4; ++i) {
          a0 = mfma(wof[4 * c + i], f[c & 1][2 * i], a0);
          a1 = mfma(wof[4 * c + i], f[c & 1][2 * i + 1], a1);
        }
        __builtin_amdgcn_sched_barrier(0);
      }
      char LAS* tp = sm + L_SH + tok * 272 + 32 * w + 8 * g4;
      *(v2u LAS*)tp = packacc(a0);
      *(v2u LAS*)(tp + 16 * 272) = packacc(a1);
      BAR();
      const v4u v = *(const v4u LAS*)(sm + L_SH + (4 * w + g4) * 272 + tok * 16);
      ST128(v, (128 * t + 32 * rq + 4 * w + g4) * 1024 + cq * 256 + tok * 16, OFF_AA + r * (2u << 20));
    }
    STAMP(1 + r, 5);
    v4u xo;
    {
      IDS
      SLEEP(10);
      v4u av;
      _Pragma("unroll 1") for (unsigned spins = 0; spins < SPIN; ++spins) {
        av = LD128(lane16, OFF_AA + r * (2u << 20) + myrow * 1024);
        if (__all(valid4(av))) break;
        SLEEP(2);
      }
      STAMP(1 + r, 6);
      f32x2 x1[4];
      float mb, ri;
#pragma unroll
      for (int e = 0; e < 4; ++e) x1[e] = unp(pack2(unp(xres[e]) + unp(av[e])));
      const v4u y = ln_row(x1, mb, ri);
      const float d = wave_sum(dot2(y[3], wrv[3], dot2(y[2], wrv[2], dot2(y[1], wrv[1], dot2(y[0], wrv[0], 0.f)))));
      unsigned u = __float_as_uint(rbf(d)) >> 16;
      u = (u & 0x8000u) ? (u ^ 0xffffu) : (u ^ 0x8000u);
      v4u rec = {(u << 16) | (unsigned)(0xfffe - myrow), __float_as_uint(mb), __float_as_uint(ri), 0u};
      rec[1] = rec[1] == SENT ? 0x7fc00000u : rec[1];
      rec[2] = rec[2] == SENT ? 0x7fc00000u : rec[2];
      if (l == 0) *(v4u LAS*)(sm + L_SH + 8960 + w * 16) = rec;
      BAR();
      if (tid < 8) ST128(*(const v4u LAS*)(sm + L_SH + 8960 + tid * 16), (128 * t + 8 * j + tid) * 16, SM_REC + r * 32768);
      STAMP(1 + r, 7);
#pragma unroll
      for (int e = 0; e < 4; ++e) xo[e] = pack2(x1[e] + unp(y[e]));
      if (r == 0) {
        xres = xo;
        ST128(xo, lane16, OFF_X2 + myrow * 1024);
#pragma unroll
        for (int e = 0; e < 4; ++e) x1[e] = unp(xo[e]);
        *(v4u LAS*)(sm + L_SH + 9216 + w * 1040 + lane16) = ln_row(x1, mb, ri);
        BAR();
        const int chunk = 8 * w + (l >> 3), r8 = l & 7;
        const v4u v = *(const v4u LAS*)(sm + L_SH + 9216 + r8 * 1040 + chunk * 16);
        ST128(v, (((8 * t + (j >> 1)) * 16 + (chunk >> 2)) * 64 + (chunk & 3) * 16 + 8 * (j & 1) + r8) * 16, OFF_Z2);
        const v4u sv = {SENT, SENT, SENT, SENT};
        ST128(sv, lane16, OFF_OF + (2u << 20) + ((8 * t + w) * 16 + j) * 1024);
        ST128(sv, (128 * t + 32 * rq + 4 * w + g4) * 1024 + cq * 256 + tok * 16, OFF_AA + (2u << 20));
        if (role != 1) {
          VMWAIT(0);
          BAR();
          if (tid == 0) __builtin_amdgcn_raw_buffer_store_b64((v2u){tag_lo + 1, tag_hi}, rws, (16 * t + j) * 128, FL_A + 32768, 16);
        }
      }
    }
    STAMP(1 + r, 8);
    if (role == 1) {
      IDS
      unsigned LAS* scr = (unsigned LAS*)(sm + L_OS);
      v4u LAS* picks = (v4u LAS*)(sm + L_OS + 1024) + 8 * w;
      v4u LAS* cand = (v4u LAS*)(sm + L_OS + 2048);
      unsigned LAS* ckey = (unsigned LAS*)(sm + L_SH + 17664);
      if (tid == 0) scr[8] = 0u;
      if (tid < 64) ckey[tid] = 0u;
      v4u ffw[4];
#pragma unroll
      for (int i = 0; i < 4; ++i) ffw[i] = LD128(lane16, OFF_GUF + (gu_u * 16 + 2 * w) * 2048 + i * 1024);
      v4u rc[4];
      _Pragma("unroll 1") for (unsigned spins = 0; spins < SPIN; ++spins) {
#pragma unroll
        for (int e = 0; e < 4; ++e) rc[e] = LD128(tid * 16, SM_REC + r * 32768 + e * 8192);
        const bool done = __all(max(max(umax4(rc[0]), umax4(rc[1])), max(umax4(rc[2]), umax4(rc[3]))) != SENT);
        if (r == 0 && spins == 0) {
          VMWAIT(0);
          BAR();
          if (tid == 0) __builtin_amdgcn_raw_buffer_store_b64((v2u){tag_lo + 1, tag_hi}, rws, (16 * t + j) * 128, FL_A + 32768, 16);
        }
        if (done) break;
        SLEEP(3);
      }
      unsigned m = max(max(rc[0][0], rc[1][0]), max(rc[2][0], rc[3][0]));
      m = wave_max(m);
      if (l == 0) scr[w] = m;
      BAR();
      STAMP(1 + r, 9);
      const v4u m0 = *(const v4u LAS*)scr, m1 = *(const v4u LAS*)(scr + 4);
      const unsigned T0 = min(min(min(m0[0], m0[1]), min(m0[2], m0[3])), min(min(m1[0], m1[1]), min(m1[2], m1[3])));
      unsigned long long bm[4];
      int tot = 0;
#pragma unroll
      for (int e = 0; e < 4; ++e) { bm[e] = __ballot(rc[e][0] >= T0); tot += __popcll(bm[e]); }
      unsigned base = 0;
      if (l == 0) base = __hip_atomic_fetch_add(scr + 8, (unsigned)tot, __ATOMIC_RELAXED, __HIP_MEMORY_SCOPE_WORKGROUP);
      base = __builtin_amdgcn_readfirstlane(base);
#pragma unroll
      for (int e = 0; e < 4; ++e) {
        const unsigned pos = base + __builtin_amdgcn_mbcnt_hi((unsigned)(bm[e] >> 32), __builtin_amdgcn_mbcnt_lo((unsigned)bm[e], 0u));
        if (rc[e][0] >= T0) { cand[pos] = rc[e]; ckey[pos] = rc[e][0]; }
        base += __popcll(bm[e]);
      }
      BAR();
      const int n = __builtin_amdgcn_readfirstlane((int)scr[8]);
      if (n <= 64) {
        const v4u mine = cand[l];
        int rank = 0;
        for (int i = 0; i < n; i += 16) {
          v4u kk[4];
#pragma unroll
          for (int e = 0; e < 4; ++e) kk[e] = *(const v4u LAS*)(ckey + i + 4 * e);
#pragma unroll
          for (int e = 0; e < 16; ++e) rank += kk[e >> 2][e & 3] > mine[0] ? 1 : 0;
        }
        if (l < n && rank < 8) picks[rank] = mine;
      } else {
        for (int c0 = 0; c0 < n; c0 += 64) {
          if (c0 + l < n) {
            const v4u mine = cand[c0 + l];
            int rank = 0;
            for (int i = 0; i < n; ++i) rank += ckey[i] > mine[0] ? 1 : 0;
            if (rank < 8) picks[rank] = mine;
          }
        }
      }
      asm volatile("s_waitcnt lgkmcnt(0)" ::: "memory");
      const v4u my = picks[l & 7];
      const int st = (0xfffe - (int)(my[0] & 0xffffu)) & 2047;
      STAMP(1 + r, 10);
      v4u xa[2], aa[2];
      const unsigned ro = (unsigned)(st * 1024 + 128 * w + 16 * g4);
      _Pragma("unroll 1") for (unsigned spins = 0; spins < SPIN; ++spins) {
#pragma unroll
        for (int i = 0; i < 2; ++i) {
          xa[i] = r == 0 ? *(const v4u*)((const char*)X + ro + 64 * i) : LD128(ro + 64 * i, OFF_X2);
          aa[i] = LD128(ro + 64 * i, OFF_AA + r * (2u << 20));
        }
        if (spins == 0 && gu_u == 0 && w == 0) {
          ST128(my, lane16, SM_IDX + r * 1024);
          if (r == 0 && l == 0)
            __builtin_amdgcn_raw_buffer_store_b64((v2u){ep_lo + 1u, ep_hi + (ep_lo == 0xffffffffu ? 1u : 0u)}, rws, 0, FL_E, 16);
        }
        if (__all(max(umax4(aa[0]), umax4(aa[1])) != SENT)) break;
        SLEEP(2);
      }
      STAMP(1 + r, 11);
      const float mb = __uint_as_float(my[1]), ri = __uint_as_float(my[2]);
      f32x4 ag = {0, 0, 0, 0}, au = {0, 0, 0, 0};
#pragma unroll
      for (int i = 0; i < 2; ++i) {
        v4u y;
#pragma unroll
        for (int e = 0; e < 4; ++e) y[e] = norm2(unp(pack2(unp(xa[i][e]) + unp(aa[i][e]))), mb, ri);
        ag = mfma(ffw[2 * i], y, ag);
        au = mfma(ffw[2 * i + 1], y, au);
      }
      f32x4 LAS* red = (f32x4 LAS*)(sm + L_SH);
      if (tok < 8) { red[w * 64 + g4 * 8 + tok] = ag; red[w * 64 + 32 + g4 * 8 + tok] = au; }
      BAR();
      if (w == 0) {
        const f32x2 LAS* rd = (const f32x2 LAS*)red + l;
        f32x2 gt = rd[0], up = rd[64];
#pragma unroll
        for (int i = 1; i < 8; ++i) { gt += rd[i * 128]; up += rd[i * 128 + 64]; }
        const f32x2 gv = unp(pack2(gt)), uv = unp(pack2(up));
        f32x2 sg;
#pragma unroll
        for (int e = 0; e < 2; ++e) sg[e] = gv[e] * __builtin_amdgcn_rcpf(1.0f + __expf(-gv[e]));
        const unsigned o = pack2(unp(pack2(sg)) * uv);
        __builtin_amdgcn_raw_buffer_store_b32(o, rws, gu_u * 256 + (l >> 5) * 128 + ((l >> 1) & 7) * 16 + ((l >> 4) & 1) * 8 + (l & 1) * 4,
                                              SM_HF + r * 32768, 16);
      }
      BAR();
    }
    STAMP(1 + r, 12);
    if (r == 0) qkv_gemm<1>(sm, rws, w0, tag_lo, tag_hi, Wr, Wg, Wu, Wd, wrv, wof, fv);
    STAMP(1 + r, 13);
    v4u srec;
    {
      IDS
      v4u LAS* scr = (v4u LAS*)(sm + L_SH + 32000);
      if (w == 7) {
        v4u f;
        _Pragma("unroll 1") for (unsigned spins = 0; spins < SPIN; ++spins) {
          f = LD128(xcd * 128 + (l & 7) * 16, SM_IDX + r * 1024);
          if (__all(valid4(f))) break;
          SLEEP(4);
        }
        if (l < 8) scr[l] = f;
      }
      BAR();
      srec = scr[l & 7];
    }
    const int stok = (0xfffe - (int)(srec[0] & 0xffffu)) & 2047;
    if (role == 2) {
      IDS
      v4u ffw[7];
#pragma unroll
      for (int i = 0; i < 7; ++i) ffw[i] = LD128(lane16, OFF_DNF + (dn_u * 56 + 7 * w + i) * 1024);
      v2u xa, aa;
      const unsigned ro = (unsigned)(stok * 1024 + dn_u * 32 + 8 * g4);
      _Pragma("unroll 1") for (unsigned spins = 0; spins < SPIN; ++spins) {
        xa = r == 0 ? *(const v2u*)((const char*)X + ro) : __builtin_amdgcn_raw_buffer_load_b64(rws, ro, OFF_X2, 16);
        aa = __builtin_amdgcn_raw_buffer_load_b64(rws, ro, OFF_AA + r * (2u << 20), 16);
        if (__all(max(aa[0], aa[1]) != SENT)) break;
        SLEEP(2);
      }
      v4u hf[7];
      _Pragma("unroll 1") for (unsigned spins = 0; spins < SPIN; ++spins) {
        unsigned mx = 0;
#pragma unroll
        for (int s = 0; s < 7; ++s) {
          hf[s] = LD128((7 * w + s) * 512 + g4 * 128 + (l & 7) * 16, SM_HF + r * 32768);
          mx = max(mx, umax4(hf[s]));
        }
        if (__all(mx != SENT)) break;
        SLEEP(3);
      }
      STAMP(1 + r, 14);
      f32x4 acc = {0, 0, 0, 0};
#pragma unroll
      for (int s = 0; s < 7; ++s) acc = mfma(ffw[s], hf[s], acc);
      f32x4 LAS* red = (f32x4 LAS*)(sm + L_OS + 16384);
      red[w * 64 + l] = acc;
      BAR();
      if (w == 0 && tok < 8) {
        f32x4 y = red[l];
#pragma unroll
        for (int i = 1; i < 8; ++i) y += red[i * 64 + l];
        unsigned u = srec[0] >> 16;
        u = (u & 0x8000u) ? (u ^ 0x8000u) : (u ^ 0xffffu);
        const float rr = __uint_as_float(u << 16);
        const float gt = rbf(__builtin_amdgcn_rcpf(1.0f + __expf(-rr)));
        const float mb = __uint_as_float(srec[1]), ri = __uint_as_float(srec[2]);
        v2u o;
#pragma unroll
        for (int e = 0; e < 2; ++e) {
          const f32x2 x1 = unp(pack2(unp(xa[e]) + unp(aa[e])));
          const f32x2 y1 = unp(norm2(x1, mb, ri));
          const f32x2 up = unp(pack2(unp(pack2(y[2 * e], y[2 * e + 1])) * gt));
          o[e] = pack2(x1 + unp(pack2(y1 + up)));
        }
        if (r == 0) __builtin_amdgcn_raw_buffer_store_b64(o, rws, tok * 1024 + dn_u * 32 + 8 * g4, SM_X2S, 16);
        else *(v2u*)((char*)Out + ro) = o;
      }
      BAR();
    }
    {
      IDS
      const unsigned long long own = __ballot(stok == myrow);
      if (r == 0) {
        const int lo = 128 * t - 16;
        const bool inr = stok >= lo && stok < lo + 144;
        const unsigned inmask = (unsigned)__ballot(inr) & 0xffu;
        if (inmask) {
          if ((inmask >> w) & 1) {
            v4u xv;
            _Pragma("unroll 1") for (unsigned spins = 0; spins < SPIN; ++spins) {
              xv = LD128(lane16, SM_X2S + w * 1024);
              if (__all(valid4(xv))) break;
              SLEEP(3);
            }
            f32x2 x[4] = {unp(xv[0]), unp(xv[1]), unp(xv[2]), unp(xv[3])};
            float mb, ri;
            *(v4u LAS*)(sm + L_OS + l * 128 + w * 16) = ln_row(x, mb, ri);
          }
          BAR();
          if (w < 6) {
            f32x4 acc = {0, 0, 0, 0};
#pragma unroll
            for (int s = 0; s < 16; ++s)
              acc = mfma(*(const v4u LAS*)(sm + L_W + (6 * s + w) * 1024 + lane16),
                         *(const v4u LAS*)(sm + L_OS + (4 * s + g4) * 128 + (l & 7) * 16), acc);
            if (inr && tok < 8)
              *(v2u LAS*)(sm + L_SH + (2 * w + (g4 >> 1)) * PLANE + 8 * (g4 & 1) + (stok - lo) * 16) = packacc(acc);
          }
          if (own) {
            const int slot = __builtin_ctzll(own);
            _Pragma("unroll 1") for (unsigned spins = 0; spins < SPIN; ++spins) {
              xres = LD128(lane16, SM_X2S + slot * 1024);
              if (__all(valid4(xres))) break;
              SLEEP(2);
            }
            ST128(xres, lane16, OFF_X2 + myrow * 1024);
          }
          BAR();
        }
        STAMP(2, 2);
        attention(sm, rws, w0, 1);
      } else if (!own) {
        *(v4u*)((char*)Out + myrow * 1024 + lane16) = xo;
      }
    }
    STAMP(1 + r, 15);
  }
}

extern "C" void kernel_launch(void* const* d_in, const int* in_sizes, int n_in, void* d_out, int out_size,
                              void* d_ws, size_t ws_size, hipStream_t stream) {
  (void)hipFuncSetAttribute((const void*)k_main, hipFuncAttributeMaxDynamicSharedMemorySize, L_TOTAL);
  k_main<<<256, 512, L_TOTAL, stream>>>((const u16*)d_in[0], (const u16*)d_in[1], (const u16*)d_in[2], (const u16*)d_in[4],
                                        (const u16*)d_in[5], (const u16*)d_in[6], (const u16*)d_in[7], (u16*)d_out, (char*)d_ws);
}
```

```cpp
#include <hip/hip_runtime.h>
#include <hip/hip_bf16.h>
#include <stdint.h>

#define LAS __attribute__((address_space(3)))
typedef unsigned short u16;
typedef short v4s __attribute__((ext_vector_type(4)));
typedef short v8s __attribute__((ext_vector_type(8)));
typedef __bf16 v2bf __attribute__((ext_vector_type(2)));
typedef float f32x4 __attribute__((ext_vector_type(4)));
typedef float f32x2 __attribute__((ext_vector_type(2)));
typedef float f32x16 __attribute__((ext_vector_type(16)));
typedef unsigned v4u __attribute__((ext_vector_type(4)));
typedef unsigned v2u __attribute__((ext_vector_type(2)));

#define STAMP(k, i)
#define TS(v)
#define TSPUT(k, i, v)
#define WPUT(i, v)
#define WST(i)

constexpr int S = 2048, D = 512, F = 1792;
constexpr unsigned SENT = 0xFFFFFFFFu;
constexpr unsigned SPIN = 1u << 18;
constexpr unsigned OFF_Z1 = 0, OFF_Z2 = 2u << 20, OFF_OF = 4u << 20, OFF_AA = 8u << 20, OFF_X2 = 12u << 20,
                   OFF_SM = 14u << 20, OFF_WF = 16u << 20, OFF_WOF = 18u << 20, OFF_GUF = 19u << 20,
                   OFF_DNF = 23u << 20, WS_BYTES = 25u << 20;
constexpr unsigned SM_REC = OFF_SM, SM_HF = OFF_SM + 65536, SM_X2S = OFF_SM + 131072, SM_IDX = OFF_SM + 139264;
constexpr unsigned FL_A = OFF_SM + 262144;
constexpr unsigned FL_B = FL_A + 65536;
constexpr unsigned FL_C = FL_B + 4096;
constexpr unsigned FL_E = FL_C + 4096;
constexpr int L_W = 0, L_SH = 98304, L_OS = 131072, L_TOTAL = 163840;
constexpr int PLANE = 2304;

__device__ __forceinline__ float bf_lo(unsigned u) { return __uint_as_float(u << 16); }
__device__ __forceinline__ float bf_hi(unsigned u) { return __uint_as_float(u & 0xffff0000u); }
__device__ __forceinline__ unsigned pack2(f32x2 f) {
  v2bf c = __builtin_convertvector(f, v2bf);
  return __builtin_bit_cast(unsigned, c);
}
__device__ __forceinline__ unsigned pack2(float a, float b) { return pack2((f32x2){a, b}); }
__device__ __forceinline__ f32x2 unp(unsigned u) { return (f32x2){bf_lo(u), bf_hi(u)}; }
__device__ __forceinline__ float rbf(float a) {
  __bf16 c = (__bf16)a;
  return __uint_as_float(((unsigned)__builtin_bit_cast(u16, c)) << 16);
}
__device__ __forceinline__ float dot2(unsigned a, unsigned b, float c) {
  return __builtin_amdgcn_fdot2_f32_bf16(__builtin_bit_cast(v2bf, a), __builtin_bit_cast(v2bf, b), c, false);
}
__device__ __forceinline__ v4u tr2(const char LAS* p, int second) {
  const v4s a = __builtin_amdgcn_ds_read_tr16_b64_v4i16((LAS v4s*)p);
  const v4s b = __builtin_amdgcn_ds_read_tr16_b64_v4i16((LAS v4s*)(p + second));
  const v8s r = {a[0], a[1], a[2], a[3], b[0], b[1], b[2], b[3]};
  return __builtin_bit_cast(v4u, r);
}
__device__ __forceinline__ f32x4 mfma(v4u a, v4u b, f32x4 c) {
  return __builtin_amdgcn_mfma_f32_16x16x32_bf16(__builtin_bit_cast(v8s, a), __builtin_bit_cast(v8s, b), c, 0, 0, 0);
}
__device__ __forceinline__ v2u packacc(f32x4 a) { return (v2u){pack2(a[0], a[1]), pack2(a[2], a[3])}; }
typedef __amdgpu_buffer_rsrc_t srd_t;
__device__ __forceinline__ srd_t make_srd(const void* p, unsigned bytes) {
  return __builtin_amdgcn_make_buffer_rsrc((void*)p, 0, bytes, 0x00020000);
}
__device__ __forceinline__ void dma16(srd_t srd, unsigned voff, unsigned soff, unsigned lds) {
  unsigned keep;
  asm volatile("s_mov_b32 %0, m0\n\ts_mov_b32 m0, %3\n\ts_nop 0\n\tbuffer_load_dwordx4 %1, %2, %4 offen lds\n\ts_mov_b32 m0, %0"
               : "=&s"(keep) : "v"(voff), "s"(srd), "s"(lds), "s"(soff));
}
__device__ __forceinline__ void dma16_sc1(srd_t srd, unsigned voff, unsigned soff, unsigned lds) {
  unsigned keep;
  asm volatile("s_mov_b32 %0, m0\n\ts_mov_b32 m0, %3\n\ts_nop 0\n\tbuffer_load_dwordx4 %1, %2, %4 offen sc1 lds\n\ts_mov_b32 m0, %0"
               : "=&s"(keep) : "v"(voff), "s"(srd), "s"(lds), "s"(soff));
}
__device__ __forceinline__ void aload16(v4u& d, srd_t srd, unsigned voff, unsigned soff) {
  asm volatile("buffer_load_dwordx4 %0, %1, %2, %3 offen" : "=v"(d) : "v"(voff), "s"(srd), "s"(soff));
}
__device__ __forceinline__ void aload16_sc1(v4u& d, srd_t srd, unsigned voff, unsigned soff) {
  asm volatile("buffer_load_dwordx4 %0, %1, %2, %3 offen sc1" : "=v"(d) : "v"(voff), "s"(srd), "s"(soff));
}
__device__ __forceinline__ void astore16_sc1(v4u d, srd_t srd, unsigned voff, unsigned soff) {
  asm volatile("buffer_store_dwordx4 %0, %1, %2, %3 offen sc1\n\ts_nop 1" :: "v"(d), "v"(voff), "s"(srd), "s"(soff));
}
#define SLEEP(n) do { __builtin_amdgcn_s_sleep(n); asm volatile("" ::: "memory"); } while (0)
#define VMWAIT(n) asm volatile("s_waitcnt vmcnt(" #n ")" ::: "memory")
#define BAR() do { asm volatile("s_waitcnt lgkmcnt(0)" ::: "memory"); __builtin_amdgcn_s_barrier(); asm volatile("" ::: "memory"); } while (0)
#define LD128(voff, soff) __builtin_amdgcn_raw_buffer_load_b128(rws, voff, soff, 16)
__device__ __forceinline__ void st128(v4u d, srd_t srd, unsigned voff, unsigned soff) {
  asm volatile("buffer_store_dwordx4 %0, %1, %2, %3 offen sc1\n\ts_nop 1" :: "v"(d), "v"(voff), "s"(srd), "s"(soff) : "memory");
}
#define ST128(v, voff, soff) st128(v, rws, voff, soff)
__device__ __forceinline__ unsigned umax4(v4u v) { return max(max(v[0], v[1]), max(v[2], v[3])); }
__device__ __forceinline__ bool valid4(v4u v) { return umax4(v) != SENT; }

template <int CTRL, int RMASK>
__device__ __forceinline__ float dpp_add(float v) {
  int t = __builtin_amdgcn_update_dpp(0, __float_as_int(v), CTRL, RMASK, 0xf, true);
  return v + __int_as_float(t);
}
__device__ __forceinline__ float wave_sum(float v) {
  v = dpp_add<0x111, 0xf>(v); v = dpp_add<0x112, 0xf>(v); v = dpp_add<0x114, 0xf>(v); v = dpp_add<0x118, 0xf>(v);
  v = dpp_add<0x142, 0xa>(v); v = dpp_add<0x143, 0xc>(v);
  return __int_as_float(__builtin_amdgcn_readlane(__float_as_int(v), 63));
}
template <int CTRL, int RMASK>
__device__ __forceinline__ unsigned dpp_max(unsigned v) {
  unsigned t = (unsigned)__builtin_amdgcn_update_dpp(0, (int)v, CTRL, RMASK, 0xf, true);
  return v > t ? v : t;
}
__device__ __forceinline__ unsigned wave_max(unsigned v) {
  v = dpp_max<0x111, 0xf>(v); v = dpp_max<0x112, 0xf>(v); v = dpp_max<0x114, 0xf>(v); v = dpp_max<0x118, 0xf>(v);
  v = dpp_max<0x142, 0xa>(v); v = dpp_max<0x143, 0xc>(v);
  return (unsigned)__builtin_amdgcn_readlane((int)v, 63);
}
__device__ __forceinline__ unsigned norm2(f32x2 x, float mb, float ri) { return pack2(unp(pack2(x - mb)) * ri); }
__device__ __forceinline__ v4u ln_row(const f32x2 (&x)[4], float& mb, float& ri) {
  const float c = __int_as_float(__builtin_amdgcn_readfirstlane(__float_as_int(x[0][0])));
  const f32x2 d0 = x[0] - c, d1 = x[1] - c, d2 = x[2] - c, d3 = x[3] - c;
  const f32x2 s2 = (d0 + d1) + (d2 + d3);
  const f32x2 q2 = (d0 * d0 + d1 * d1) + (d2 * d2 + d3 * d3);
  float s = s2[0] + s2[1], q = q2[0] + q2[1];
  s = dpp_add<0x111, 0xf>(s); q = dpp_add<0x111, 0xf>(q);
  s = dpp_add<0x112, 0xf>(s); q = dpp_add<0x112, 0xf>(q);
  s = dpp_add<0x114, 0xf>(s); q = dpp_add<0x114, 0xf>(q);
  s = dpp_add<0x118, 0xf>(s); q = dpp_add<0x118, 0xf>(q);
  s = dpp_add<0x142, 0xa>(s); q = dpp_add<0x142, 0xa>(q);
  s = dpp_add<0x143, 0xc>(s); q = dpp_add<0x143, 0xc>(q);
  const float dm = __int_as_float(__builtin_amdgcn_readlane(__float_as_int(s), 63)) * (1.0f / D);
  const float var = fmaxf(__int_as_float(__builtin_amdgcn_readlane(__float_as_int(q), 63)) * (1.0f / D) - dm * dm, 0.f);
  mb = rbf(c + dm);
  const float sg = rbf(__builtin_amdgcn_sqrtf(rbf(rbf(var) + 1e-5f)));
  ri = __builtin_amdgcn_rcpf(sg);
  return (v4u){norm2(x[0], mb, ri), norm2(x[1], mb, ri), norm2(x[2], mb, ri), norm2(x[3], mb, ri)};
}

#define IDS                                                                                              \
  int tid = threadIdx.x, w = w0, b = blockIdx.x;                                                         \
  asm volatile("" : "+v"(tid)); asm volatile("" : "+s"(w)); asm volatile("" : "+s"(b));                  \
  const int l = tid & 63, xcd = b & 7, jj = b >> 3;                                                      \
  const int t = 4 * (xcd >> 1) + (jj >> 3), j = 8 * (xcd & 1) + (jj & 7);                                \
  const int g4 = l >> 4, tok = l & 15;                                                                   \
  const int rq = j >> 2, cq = j & 3, myrow = 128 * t + 8 * j + w;                                        \
  const int gu_u = 14 * xcd + jj, dn_u = 4 * xcd + (jj - 14);                                            \
  const unsigned lane16 = l * 16;                                                                        \
  const int rth = t > 0 ? 8 * t - 1 : 0;                                                                 \
  (void)rq; (void)cq; (void)myrow; (void)gu_u; (void)dn_u; (void)lane16; (void)rth; (void)tok; (void)g4;

__device__ __forceinline__ void attention(char LAS* sm, srd_t rws, int w0, int r) {
  IDS
  const char LAS* rp = sm + L_SH + g4 * PLANE + (16 + 16 * w + tok) * 16;
  const v4u q0 = *(const v4u LAS*)rp;
  float d[8];
#pragma unroll
  for (int k = 0; k < 8; ++k) {
    const v4u k0 = *(const v4u LAS*)(rp + 4 * PLANE - 32 * k);
    d[k] = dot2(q0[3], k0[3], dot2(q0[2], k0[2], dot2(q0[1], k0[1], dot2(q0[0], k0[0], 0.f))));
  }
  float lg[4];
#pragma unroll
  for (int k = 0; k < 4; ++k) {
    const auto sw = __builtin_amdgcn_permlane16_swap(__float_as_uint(d[k]), __float_as_uint(d[k + 4]), false, false);
    lg[k] = rbf(__uint_as_float(sw[0]) + __uint_as_float(sw[1]));
  }
  if (t == 0) {
#pragma unroll
    for (int k = 0; k < 4; ++k) lg[k] = (16 * w + tok - 2 * k - 8 * (g4 & 1) >= 0) ? lg[k] : -4e9f;
  }
  float m = fmaxf(fmaxf(lg[0], lg[1]), fmaxf(lg[2], lg[3]));
  {
    const auto sw = __builtin_amdgcn_permlane16_swap(__float_as_uint(m), __float_as_uint(m), false, false);
    m = fmaxf(__uint_as_float(sw[0]), __uint_as_float(sw[1]));
  }
  constexpr float C = 0.25f * 1.44269504088896341f;
#pragma unroll
  for (int k = 0; k < 4; ++k) lg[k] = __builtin_amdgcn_exp2f((lg[k] - m) * C);
  float sum = (lg[0] + lg[1]) + (lg[2] + lg[3]);
  {
    const auto sw = __builtin_amdgcn_permlane16_swap(__float_as_uint(sum), __float_as_uint(sum), false, false);
    sum = __uint_as_float(sw[0]) + __uint_as_float(sw[1]);
  }
  const float inv = __builtin_amdgcn_rcpf(sum);
  const unsigned pa = pack2(lg[0] * inv, lg[1] * inv), pb = pack2(lg[2] * inv, lg[3] * inv);
  const auto sa = __builtin_amdgcn_permlane16_swap(pa, pa, false, false);
  const auto sb = __builtin_amdgcn_permlane16_swap(pb, pb, false, false);
  const unsigned pp[4] = {sa[0], sb[0], sa[1], sb[1]};
  float o[8];
#pragma unroll
  for (int e = 0; e < 8; ++e) o[e] = 0.f;
#pragma unroll
  for (int k = 0; k < 4; ++k) {
    const v4u v0 = *(const v4u LAS*)(rp + 8 * PLANE - 64 * k), v1 = *(const v4u LAS*)(rp + 8 * PLANE - 64 * k - 32);
#pragma unroll
    for (int e = 0; e < 4; ++e) {
      o[2 * e] = dot2(pp[k], __builtin_amdgcn_perm(v1[e], v0[e], 0x05040100u), o[2 * e]);
      o[2 * e + 1] = dot2(pp[k], __builtin_amdgcn_perm(v1[e], v0[e], 0x07060302u), o[2 * e + 1]);
    }
  }
  const v4u ov = {pack2(o[0], o[1]), pack2(o[2], o[3]), pack2(o[4], o[5]), pack2(o[6], o[7])};
  ST128(ov, lane16, OFF_OF + r * (2u << 20) + ((8 * t + w) * 16 + j) * 1024);
}

struct Unit { srd_t srd; unsigned stride, dst, dstep, dodd; };
__device__ __forceinline__ Unit ffn_unit(int u, const u16* Wg, const u16* Wu, const u16* Wd) {
  Unit r;
  const char* src = (const char*)Wd;
  r.stride = 1024; r.dst = WS_BYTES; r.dstep = 0; r.dodd = 0;
  if (u < 896) {
    const int up = u >= 448, v = u - 448 * up, s = v / 28, nb = v % 28;
    src = (const char*)(up ? Wu : Wg) + (size_t)(32 * s) * (F * 2) + nb * 128; r.stride = F * 2;
    r.dst = OFF_GUF + ((4 * nb * 16 + s) * 2 + up) * 1024; r.dstep = 2 * 32 * 1024; r.dodd = 32 * 1024;
  } else if (u < 1344) {
    const int s = (u - 896) >> 3, nb = u & 7;
    src = (const char*)Wd + (size_t)(32 * s) * 1024 + nb * 128;
    r.dst = OFF_DNF + (4 * nb * 56 + s) * 1024; r.dstep = 2 * 56 * 1024; r.dodd = 56 * 1024;
  }
  r.srd = make_srd(src, u < 1344 ? 32 * F * 2 : 0);
  return r;
}
__device__ __forceinline__ void unit_store(char LAS* scr, srd_t rws, int l, const v4u (&wv)[4], unsigned dst, unsigned dstep, unsigned dodd) {
#pragma unroll
  for (int i = 0; i < 4; ++i) *(v4u LAS*)(scr + i * 1024 + l * 16) = wv[i];
  asm volatile("s_waitcnt lgkmcnt(0)" ::: "memory");
  const char LAS* p = scr + (8 * (l >> 4) + ((l & 15) >> 2)) * 128 + 8 * (l & 3);
  v4s a[4], c[4];
#pragma unroll
  for (int i = 0; i < 4; ++i) {
    a[i] = __builtin_amdgcn_ds_read_tr16_b64_v4i16((LAS v4s*)(p + 32 * i));
    c[i] = __builtin_amdgcn_ds_read_tr16_b64_v4i16((LAS v4s*)(p + 32 * i + 512));
  }
#pragma unroll
  for (int i = 0; i < 4; ++i) {
    const v8s f = {a[i][0], a[i][1], a[i][2], a[i][3], c[i][0], c[i][1], c[i][2], c[i][3]};
    astore16_sc1(__builtin_bit_cast(v4u, f), rws, l * 16, dst + (i >> 1) * dstep + (i & 1) * dodd);
  }
  asm volatile("" ::: "memory");
}
__device__ __forceinline__ void wait_all_flags(srd_t rws, int l, unsigned off, unsigned lo, unsigned hi) {
  _Pragma("unroll 1") for (unsigned spins = 0; spins < SPIN; ++spins) {
    const v4u a = LD128(l * 32, off), c = LD128(l * 32 + 16, off);
    if (__all((((a[0] ^ lo) | (a[1] ^ hi)) | ((a[2] ^ lo) | (a[3] ^ hi)) | ((c[0] ^ lo) | (c[1] ^ hi)) | ((c[2] ^ lo) | (c[3] ^ hi))) == 0u)) break;
    SLEEP(3);
  }
}
constexpr int L_CTL = L_SH + 32704;
#define WFRAG(s, ct) ((6 * (s) + (ct)) * 1024)

#define WAIT_TIE4(n, a, i) do { VMWAIT(n); __builtin_amdgcn_sched_barrier(0); asm volatile("" : "+v"(a[i]), "+v"(a[i + 1]), "+v"(a[i + 2]), "+v"(a[i + 3])); } while (0)
template <int R>
__device__ __forceinline__ void qkv_gemm(char LAS* sm, srd_t rws, int w0, unsigned tag_lo, unsigned tag_hi,
                                         const u16* Wg, const u16* Wu, const u16* Wd) {
  IDS
  if (R == 1 && tid == 64) *(unsigned LAS*)(sm + L_CTL) = 0u;
  if (w == 0) {
    int fi = l < 16 ? 16 * t + l : (t > 0 ? 16 * t - 2 + (l & 1) : 0);
    if (R == 0 && l >= 32) fi = 16 * ((l >> 1) & 15) + (j & 14) + (l & 1);
    _Pragma("unroll 1") for (unsigned spins = 0; spins < SPIN; ++spins) {
      const v2u f = __builtin_amdgcn_raw_buffer_load_b64(rws, fi * 128, FL_A + R * 32768, 16);
      if (__all(((f[0] ^ (tag_lo + R)) | (f[1] ^ tag_hi)) == 0u)) break;
      SLEEP(3);
    }
  }
  BAR();
  STAMP(3, 4 + 4 * R);
  unsigned long long ta, tb, tc, td;
  TS(ta);
  const unsigned zo = R ? OFF_Z2 : OFF_Z1;
  if (w < 4) {
    const unsigned lo = (unsigned)(((l >> 4) & 1) * 16384 + (l >> 5) * 256 + tok * 16);
    const unsigned zb = zo + (8 * t + 2 * w) * 16384;
    v4u z[32];
#pragma unroll
    for (int u = 0; u < 8; ++u) aload16_sc1(z[u], rws, lo, zb + u * 512);
    const char LAS* wp = sm + L_W + ((l >> 4) & 1) * 1024 + (l >> 5) * 256 + tok * 16;
    f32x16 acc[3];
#pragma unroll
    for (int c = 0; c < 3; ++c)
#pragma unroll
      for (int e = 0; e < 16; ++e) acc[c][e] = 0.f;
    v4u f[3][3];
    TS(tb);
    if (R == 0) BAR();
    TS(tc);
#pragma unroll
    for (int u = 8; u < 12; ++u) aload16_sc1(z[u], rws, lo, zb + u * 512);
#pragma unroll
    for (int u = 0; u < 2; ++u)
#pragma unroll
      for (int c = 0; c < 3; ++c) f[u][c] = *(const v4u LAS*)(wp + (6 * (u >> 1) + 2 * c) * 1024 + (u & 1) * 512);
#pragma unroll
    for (int u = 0; u < 32; ++u) {
      if (!(u & 3)) { if (u < 24) WAIT_TIE4(8, z, u); else if (u == 24) WAIT_TIE4(4, z, 24); else WAIT_TIE4(0, z, 28); }
      if (R == 0 && (u & 7) == 6 && u < 30) { asm volatile("" ::: "memory"); __builtin_amdgcn_s_barrier(); asm volatile("" ::: "memory"); }
      if (u < 30) {
#pragma unroll
        for (int c = 0; c < 3; ++c)
          f[(u + 2) % 3][c] = *(const v4u LAS*)(wp + (6 * ((u + 2) >> 1) + 2 * c) * 1024 + (u & 1) * 512);
      }
      __builtin_amdgcn_sched_barrier(0);
#pragma unroll
      for (int c = 0; c < 3; ++c)
        acc[c] = __builtin_amdgcn_mfma_f32_32x32x16_bf16(__builtin_bit_cast(v8s, f[u % 3][c]), __builtin_bit_cast(v8s, z[u]), acc[c], 0, 0, 0);
      if (u < 20) aload16_sc1(z[u + 12], rws, lo, zb + (u + 12) * 512);
      __builtin_amdgcn_sched_barrier(0);
    }
    TS(td);
    WPUT(4 * R, ta); WPUT(4 * R + 1, tb); WPUT(4 * R + 2, tc); WPUT(4 * R + 3, td);
    BAR();
    char LAS* qp = sm + L_SH + (16 + 32 * w + (l & 31)) * 16 + 8 * (l >> 5);
#pragma unroll
    for (int c = 0; c < 3; ++c)
#pragma unroll
      for (int g = 0; g < 4; ++g)
        *(v2u LAS*)(qp + (4 * c + g) * PLANE) =
            (v2u){pack2(acc[c][4 * g], acc[c][4 * g + 1]), pack2(acc[c][4 * g + 2], acc[c][4 * g + 3])};
  } else {
    const int h = w - 4;
    v4u zh[4];
#pragma unroll
    for (int i = 0; i < 4; ++i) aload16_sc1(zh[i], rws, lane16, zo + (rth * 16 + 4 * h + i) * 1024);
    f32x4 acch = {0, 0, 0, 0};
    const char LAS* wh = sm + L_W + (2 + h) * 1024 + lane16;
    const char LAS* hp = sm + L_SH + lane16;
    if (R == 0) {
      const unsigned wb = OFF_WF + j * 98304 + 6 * h * 1024;
      v4u fv[2][4];
      const Unit u0 = ffn_unit(h * 256 + b, Wg, Wu, Wd), u1 = ffn_unit(1024 + h * 256 + b, Wg, Wu, Wd);
#pragma unroll
      for (int q = 0; q < 4; ++q) {
        if (q == 0) {
#pragma unroll
          for (int p = 0; p < 6; ++p) dma16_sc1(rws, lane16, wb + p * 1024, L_W + (6 * h + p) * 1024);
        }
        if (q < 3) {
#pragma unroll
          for (int p = 0; p < 6; ++p) dma16_sc1(rws, lane16, wb + (24 * (q + 1) + p) * 1024, L_W + (24 * (q + 1) + 6 * h + p) * 1024);
        }
        if (q == 2) {
#pragma unroll
          for (int i = 0; i < 4; ++i) aload16(fv[0][i], u0.srd, (unsigned)(l >> 3) * u0.stride + (l & 7) * 16, 8 * i * u0.stride);
#pragma unroll
          for (int i = 0; i < 4; ++i) aload16(fv[1][i], u1.srd, (unsigned)(l >> 3) * u1.stride + (l & 7) * 16, 8 * i * u1.stride);
        }
        if (q == 0) {
          WAIT_TIE4(6, zh, 0);
#pragma unroll
          for (int i = 0; i < 4; ++i) *(v4u LAS*)(sm + L_SH + (4 * h + i) * 1024 + lane16) = zh[i];
          TS(tb);
        } else if (q == 1) VMWAIT(6);
        else if (q == 2) VMWAIT(14);
        else VMWAIT(8);
        BAR();
        if (q == 0) {
          TS(tc);
          if (tid == 448) __builtin_amdgcn_raw_buffer_store_b64((v2u){tag_lo + 2, tag_hi}, rws, b * 8, FL_B, 16);
        }
      }
#pragma unroll
      for (int s = 0; s < 16; ++s) acch = mfma(*(const v4u LAS*)(wh + s * 6144), *(const v4u LAS*)(hp + s * 1024), acch);
      WAIT_TIE4(4, fv[0], 0);
      unit_store(sm + L_SH + 16384 + h * 4096, rws, l, fv[0], u0.dst, u0.dstep, u0.dodd);
      WAIT_TIE4(4, fv[1], 0);
      unit_store(sm + L_SH + 16384 + h * 4096, rws, l, fv[1], u1.dst, u1.dstep, u1.dodd);
    } else {
      WAIT_TIE4(0, zh, 0);
#pragma unroll
      for (int i = 0; i < 4; ++i) *(v4u LAS*)(sm + L_SH + (4 * h + i) * 1024 + lane16) = zh[i];
      TS(tb);
      asm volatile("s_waitcnt lgkmcnt(0)" ::: "memory");
      if (l == 0) __hip_atomic_fetch_add((unsigned LAS*)(sm + L_CTL), 1u, __ATOMIC_RELAXED, __HIP_MEMORY_SCOPE_WORKGROUP);
      volatile unsigned LAS* ctl = (volatile unsigned LAS*)(sm + L_CTL);
      _Pragma("unroll 1") for (unsigned spins = 0; spins < (SPIN << 4) && *ctl < 4u; ++spins) SLEEP(1);
      asm volatile("" ::: "memory");
      TS(tc);
#pragma unroll
      for (int s = 0; s < 16; ++s) acch = mfma(*(const v4u LAS*)(wh + s * 6144), *(const v4u LAS*)(hp + s * 1024), acch);
    }
    TS(td);
    WPUT(4 * R, ta); WPUT(4 * R + 1, tb); WPUT(4 * R + 2, tc); WPUT(4 * R + 3, td);
    BAR();
    *(v2u LAS*)(sm + L_SH + (2 * (2 + h) + (g4 >> 1)) * PLANE + 8 * (g4 & 1) + tok * 16) = packacc(acch);
  }
}

__global__ __launch_bounds__(512, 2) void k_main(const u16* __restrict__ X, const u16* __restrict__ Wqkv,
                                                 const u16* __restrict__ Wo, const u16* __restrict__ Wr,
                                                 const u16* __restrict__ Wg, const u16* __restrict__ Wu,
                                                 const u16* __restrict__ Wd, u16* __restrict__ Out,
                                                 char* __restrict__ ws) {
  extern __shared__ __attribute__((aligned(16))) char smem[];
  char LAS* sm = (char LAS*)smem;
  asm volatile("" :: "s"(X), "s"(Wqkv), "s"(Wo), "s"(Wr), "s"(Wg), "s"(Wu), "s"(Wd), "s"(Out), "s"(ws));
  const int w0 = __builtin_amdgcn_readfirstlane(threadIdx.x >> 6);
  const srd_t rws = make_srd(ws, WS_BYTES);
  const int role = (blockIdx.x >> 3) < 14 ? 1 : (blockIdx.x >> 3) < 18 ? 2 : 0;
  STAMP(1, 0);
  v4u xres, wrv;
  v4u wof[16];
  unsigned tag_lo, tag_hi, ep_lo, ep_hi;
  {
    IDS
    v2u ev;
    v4u wv[4];
    asm volatile("buffer_load_dwordx2 %0, off, %1, %2 sc1" : "=v"(ev) : "s"(rws), "s"(FL_E));
    aload16(wrv, make_srd(Wr, D * 2), lane16, 0);
    aload16(xres, make_srd(X, S * D * 2), lane16, myrow * 1024);
    const bool has_unit = w == 0 || (w == 1 && !(j & 1));
    const int up = w == 0 ? (j & 1) : 2;
    {
      const srd_t su = make_srd((const char*)Wqkv + (size_t)(32 * t) * 3072 + up * 1024 + (j >> 1) * 128, has_unit ? 31 * 3072 + 128 : 0);
#pragma unroll
      for (int i = 0; i < 4; ++i) aload16(wv[i], su, (unsigned)(l >> 3) * 3072 + (l & 7) * 16, 8 * i * 3072);
    }
    const srd_t srd_wo = make_srd(Wo, D * D * 2);
    const unsigned wovoff = (unsigned)(g4 * 1024 + ((tok ^ ((g4 | (((w >> 1) & 1) << 2)) << 1)) * 16));
#pragma unroll
    for (int q = 0; q < 2; ++q)
#pragma unroll
      for (int m = 0; m < 4; ++m)
        dma16(srd_wo, wovoff, (unsigned)((128 * q + 4 * (w + 8 * m)) * 1024 + cq * 256), L_W + q * 32768 + (w + 8 * m) * 1024);
    if (w == 0) {
#pragma unroll
      for (int m = 0; m < 4; ++m) dma16(srd_wo, wovoff, (unsigned)((256 + 32 * m) * 1024 + cq * 256), L_OS + 8 * m * 1024);
      VMWAIT(16);
    } else {
      VMWAIT(12);
    }
    __builtin_amdgcn_sched_barrier(0);
    asm volatile("" : "+v"(ev), "+v"(xres), "+v"(wrv));
    STAMP(3, 0);
    ep_lo = __builtin_amdgcn_readfirstlane(ev[0]);
    ep_hi = __builtin_amdgcn_readfirstlane(ev[1]);
    {
      const unsigned long long e = ((unsigned long long)ep_hi << 32) | ep_lo;
      const unsigned long long tg = (e + 1) * 0x9E3779B97F4A7C15ull;
      tag_lo = (unsigned)tg & ~3u; tag_hi = (unsigned)(tg >> 32);
    }
    {
      f32x2 x[4] = {unp(xres[0]), unp(xres[1]), unp(xres[2]), unp(xres[3])};
      float mb, ri;
      *(v4u LAS*)(sm + L_SH + 8192 + w * 1040 + lane16) = ln_row(x, mb, ri);
    }
    if (w == 0) VMWAIT(12); else VMWAIT(8);
    __builtin_amdgcn_sched_barrier(0);
    asm volatile("" : "+v"(wv[0]), "+v"(wv[1]), "+v"(wv[2]), "+v"(wv[3]));
    BAR();
    if (w < 2) {
      if (has_unit) unit_store(sm + L_SH + w * 4096, rws, l, wv, OFF_WF + (((j & 14) * 16 + t) * 6 + 2 * up) * 1024, 98304, 1024);
    } else if (w >= 4) {
#pragma unroll
      for (int i = 0; i < 2; ++i) {
        const int chunk = 16 * (w - 4) + 8 * i + (l >> 3), r8 = l & 7;
        const v4u v = *(const v4u LAS*)(sm + L_SH + 8192 + r8 * 1040 + chunk * 16);
        ST128(v, (((8 * t + (j >> 1)) * 16 + (chunk >> 2)) * 64 + (chunk & 3) * 16 + 8 * (j & 1) + r8) * 16, OFF_Z1);
      }
    }
    VMWAIT(0);
    STAMP(3, 2);
    BAR();
    if (tid == 0) __builtin_amdgcn_raw_buffer_store_b64((v2u){tag_lo, tag_hi}, rws, (16 * t + j) * 128, FL_A, 16);
    if (w) {
#pragma unroll
      for (int m = 0; m < 4; ++m)
        dma16(srd_wo, wovoff, (unsigned)((256 + 4 * (w + 8 * m)) * 1024 + cq * 256), L_OS + (w + 8 * m) * 1024);
    }
    const char LAS* wp = sm + (8 * g4 + (tok >> 2)) * 256 + ((w ^ ((tok >> 2) | ((g4 & 1) << 2))) * 32) + 8 * (l & 3);
#pragma unroll
    for (int s = 0; s < 8; ++s) wof[s] = tr2(wp + L_W + s * 8192, 1024);
    if (w >= 4) {
      const v4u sv = {SENT, SENT, SENT, SENT};
      const int w2 = 2 * (w - 4);
      const unsigned ao = (unsigned)((128 * t + 32 * rq + 4 * w2 + g4) * 1024 + cq * 256 + tok * 16);
      astore16_sc1(sv, rws, lane16, OFF_OF + ((8 * t + w2) * 16 + j) * 1024);
      astore16_sc1(sv, rws, lane16, OFF_OF + ((8 * t + w2 + 1) * 16 + j) * 1024);
      astore16_sc1(sv, rws, ao, OFF_AA);
      astore16_sc1(sv, rws, ao, OFF_AA + 4096);
      if (w == 4) astore16_sc1(sv, rws, l < 36 ? lane16 : WS_BYTES, OFF_SM + b * 576);
    }
    STAMP(3, 3);
  }
  qkv_gemm<0>(sm, rws, w0, tag_lo, tag_hi, Wg, Wu, Wd);
  {
    IDS
    const char LAS* wp = sm + L_OS + (8 * g4 + (tok >> 2)) * 256 + ((w ^ ((tok >> 2) | ((g4 & 1) << 2))) * 32) + 8 * (l & 3);
#pragma unroll
    for (int s = 8; s < 12; ++s) wof[s] = tr2(wp + (s - 8) * 8192, 1024);
    BAR();
    const srd_t srd_wo = make_srd(Wo, D * D * 2);
    const unsigned wovoff = (unsigned)(g4 * 1024 + ((tok ^ ((g4 | (((w >> 1) & 1) << 2)) << 1)) * 16));
#pragma unroll
    for (int m = 0; m < 4; ++m)
      dma16(srd_wo, wovoff, (unsigned)((384 + 4 * (w + 8 * m)) * 1024 + cq * 256), L_OS + (w + 8 * m) * 1024);
    STAMP(1, 2);
    attention(sm, rws, w0, 0);
    VMWAIT(1);
    BAR();
#pragma unroll
    for (int s = 12; s < 16; ++s) wof[s] = tr2(wp + (s - 12) * 8192, 1024);
    BAR();
  }

#pragma unroll 1
  for (int r = 0; r < 2; ++r) {
    STAMP(1 + r, 3);
    {
      IDS
      v4u of[4];
      const unsigned ofb = OFF_OF + r * (2u << 20) + ((8 * t + 2 * rq) * 16 + 4 * w) * 1024;
      if (r == 0) wait_all_flags(rws, l, FL_B, tag_lo + 2, tag_hi);
      else SLEEP(10);
      _Pragma("unroll 1") for (unsigned spins = 0; spins < SPIN; ++spins) {
#pragma unroll
        for (int i = 0; i < 4; ++i) of[i] = LD128(lane16, ofb + i * 1024);
        if (__all(max(max(umax4(of[0]), umax4(of[1])), max(umax4(of[2]), umax4(of[3]))) != SENT)) break;
        SLEEP(2);
      }
#pragma unroll
      for (int i = 0; i < 4; ++i) *(v4u LAS*)(sm + L_OS + (4 * w + i) * 1024 + lane16) = of[i];
    }
    BAR();
    STAMP(1 + r, 4);
    {
      IDS
      f32x4 a0 = {0, 0, 0, 0}, a1 = {0, 0, 0, 0};
      const char LAS* op = sm + L_OS + lane16;
      v4u f[2][8];
#pragma unroll
      for (int i = 0; i < 4; ++i) { f[0][2 * i] = *(const v4u LAS*)(op + i * 1024); f[0][2 * i + 1] = *(const v4u LAS*)(op + (16 + i) * 1024); }
#pragma unroll
      for (int c = 0; c < 4; ++c) {
        if (c < 3) {
#pragma unroll
          for (int i = 0; i < 4; ++i) {
            f[(c + 1) & 1][2 * i] = *(const v4u LAS*)(op + (4 * c + 4 + i) * 1024);
            f[(c + 1) & 1][2 * i + 1] = *(const v4u LAS*)(op + (20 + 4 * c + i) * 1024);
          }
        }
        __builtin_amdgcn_sched_barrier(0);
#pragma unroll
        for (int i = 0; i < 4; ++i) {
          a0 = mfma(wof[4 * c + i], f[c & 1][2 * i], a0);
          a1 = mfma(wof[4 * c + i], f[c & 1][2 * i + 1], a1);
        }
        __builtin_amdgcn_sched_barrier(0);
      }
      char LAS* tp = sm + L_SH + tok * 272 + 32 * w + 8 * g4;
      *(v2u LAS*)tp = packacc(a0);
      *(v2u LAS*)(tp + 16 * 272) = packacc(a1);
      BAR();
      const v4u v = *(const v4u LAS*)(sm + L_SH + (4 * w + g4) * 272 + tok * 16);
      ST128(v, (128 * t + 32 * rq + 4 * w + g4) * 1024 + cq * 256 + tok * 16, OFF_AA + r * (2u << 20));
    }
    STAMP(1 + r, 5);
    v4u xo;
    {
      IDS
      SLEEP(10);
      v4u av;
      _Pragma("unroll 1") for (unsigned spins = 0; spins < SPIN; ++spins) {
        av = LD128(lane16, OFF_AA + r * (2u << 20) + myrow * 1024);
        if (__all(valid4(av))) break;
        SLEEP(2);
      }
      STAMP(1 + r, 6);
      f32x2 x1[4];
      float mb, ri;
#pragma unroll
      for (int e = 0; e < 4; ++e) x1[e] = unp(pack2(unp(xres[e]) + unp(av[e])));
      const v4u y = ln_row(x1, mb, ri);
      const float d = wave_sum(dot2(y[3], wrv[3], dot2(y[2], wrv[2], dot2(y[1], wrv[1], dot2(y[0], wrv[0], 0.f)))));
      unsigned u = __float_as_uint(rbf(d)) >> 16;
      u = (u & 0x8000u) ? (u ^ 0xffffu) : (u ^ 0x8000u);
      v4u rec = {(u << 16) | (unsigned)(0xfffe - myrow), __float_as_uint(mb), __float_as_uint(ri), 0u};
      rec[1] = rec[1] == SENT ? 0x7fc00000u : rec[1];
      rec[2] = rec[2] == SENT ? 0x7fc00000u : rec[2];
      if (l == 0) *(v4u LAS*)(sm + L_SH + 8960 + w * 16) = rec;
      BAR();
      if (tid < 8) ST128(*(const v4u LAS*)(sm + L_SH + 8960 + tid * 16), (128 * t + 8 * j + tid) * 16, SM_REC + r * 32768);
      STAMP(1 + r, 7);
#pragma unroll
      for (int e = 0; e < 4; ++e) xo[e] = pack2(x1[e] + unp(y[e]));
      if (r == 0) {
        xres = xo;
        ST128(xo, lane16, OFF_X2 + myrow * 1024);
#pragma unroll
        for (int e = 0; e < 4; ++e) x1[e] = unp(xo[e]);
        *(v4u LAS*)(sm + L_SH + 9216 + w * 1040 + lane16) = ln_row(x1, mb, ri);
        BAR();
        const int chunk = 8 * w + (l >> 3), r8 = l & 7;
        const v4u v = *(const v4u LAS*)(sm + L_SH + 9216 + r8 * 1040 + chunk * 16);
        ST128(v, (((8 * t + (j >> 1)) * 16 + (chunk >> 2)) * 64 + (chunk & 3) * 16 + 8 * (j & 1) + r8) * 16, OFF_Z2);
        const v4u sv = {SENT, SENT, SENT, SENT};
        ST128(sv, lane16, OFF_OF + (2u << 20) + ((8 * t + w) * 16 + j) * 1024);
        ST128(sv, (128 * t + 32 * rq + 4 * w + g4) * 1024 + cq * 256 + tok * 16, OFF_AA + (2u << 20));
        if (role != 1) {
          VMWAIT(0);
          BAR();
          if (tid == 0) __builtin_amdgcn_raw_buffer_store_b64((v2u){tag_lo + 1, tag_hi}, rws, (16 * t + j) * 128, FL_A + 32768, 16);
        }
      }
    }
    STAMP(1 + r, 8);
    if (role == 1) {
      IDS
      unsigned LAS* scr = (unsigned LAS*)(sm + L_OS);
      v4u LAS* picks = (v4u LAS*)(sm + L_OS + 1024) + 8 * w;
      v4u LAS* cand = (v4u LAS*)(sm + L_OS + 2048);
      unsigned LAS* ckey = (unsigned LAS*)(sm + L_SH + 17664);
      if (tid == 0) scr[8] = 0u;
      if (tid < 64) ckey[tid] = 0u;
      v4u rc[4];
      _Pragma("unroll 1") for (unsigned spins = 0; spins < SPIN; ++spins) {
#pragma unroll
        for (int e = 0; e < 4; ++e) rc[e] = LD128(tid * 16, SM_REC + r * 32768 + e * 8192);
        const bool done = __all(max(max(umax4(rc[0]), umax4(rc[1])), max(umax4(rc[2]), umax4(rc[3]))) != SENT);
        if (r == 0 && spins == 0) {
          VMWAIT(0);
          BAR();
          if (tid == 0) __builtin_amdgcn_raw_buffer_store_b64((v2u){tag_lo + 1, tag_hi}, rws, (16 * t + j) * 128, FL_A + 32768, 16);
        }
        if (done) break;
        SLEEP(3);
      }
      unsigned m = max(max(rc[0][0], rc[1][0]), max(rc[2][0], rc[3][0]));
      m = wave_max(m);
      if (l == 0) scr[w] = m;
      BAR();
      STAMP(1 + r, 9);
      v4u ffw[4];
#pragma unroll
      for (int i = 0; i < 4; ++i) ffw[i] = LD128(lane16, OFF_GUF + (gu_u * 16 + 2 * w) * 2048 + i * 1024);
      const v4u m0 = *(const v4u LAS*)scr, m1 = *(const v4u LAS*)(scr + 4);
      const unsigned T0 = min(min(min(m0[0], m0[1]), min(m0[2], m0[3])), min(min(m1[0], m1[1]), min(m1[2], m1[3])));
      unsigned long long bm[4];
      int tot = 0;
#pragma unroll
      for (int e = 0; e < 4; ++e) { bm[e] = __ballot(rc[e][0] >= T0); tot += __popcll(bm[e]); }
      unsigned base = 0;
      if (l == 0) base = __hip_atomic_fetch_add(scr + 8, (unsigned)tot, __ATOMIC_RELAXED, __HIP_MEMORY_SCOPE_WORKGROUP);
      base = __builtin_amdgcn_readfirstlane(base);
#pragma unroll
      for (int e = 0; e < 4; ++e) {
        const unsigned pos = base + __builtin_amdgcn_mbcnt_hi((unsigned)(bm[e] >> 32), __builtin_amdgcn_mbcnt_lo((unsigned)bm[e], 0u));
        if (rc[e][0] >= T0) { cand[pos] = rc[e]; ckey[pos] = rc[e][0]; }
        base += __popcll(bm[e]);
      }
      BAR();
      const int n = __builtin_amdgcn_readfirstlane((int)scr[8]);
      if (n <= 64) {
        const v4u mine = cand[l];
        int rank = 0;
        for (int i = 0; i < n; i += 16) {
          v4u kk[4];
#pragma unroll
          for (int e = 0; e < 4; ++e) kk[e] = *(const v4u LAS*)(ckey + i + 4 * e);
#pragma unroll
          for (int e = 0; e < 16; ++e) rank += kk[e >> 2][e & 3] > mine[0] ? 1 : 0;
        }
        if (l < n && rank < 8) picks[rank] = mine;
      } else {
        for (int c0 = 0; c0 < n; c0 += 64) {
          if (c0 + l < n) {
            const v4u mine = cand[c0 + l];
            int rank = 0;
            for (int i = 0; i < n; ++i) rank += ckey[i] > mine[0] ? 1 : 0;
            if (rank < 8) picks[rank] = mine;
          }
        }
      }
      asm volatile("s_waitcnt lgkmcnt(0)" ::: "memory");
      const v4u my = picks[l & 7];
      const int st = (0xfffe - (int)(my[0] & 0xffffu)) & 2047;
      STAMP(1 + r, 10);
      v4u xa[2], aa[2];
      const unsigned ro = (unsigned)(st * 1024 + 128 * w + 16 * g4);
      _Pragma("unroll 1") for (unsigned spins = 0; spins < SPIN; ++spins) {
#pragma unroll
        for (int i = 0; i < 2; ++i) {
          xa[i] = r == 0 ? *(const v4u*)((const char*)X + ro + 64 * i) : LD128(ro + 64 * i, OFF_X2);
          aa[i] = LD128(ro + 64 * i, OFF_AA + r * (2u << 20));
        }
        if (spins == 0 && gu_u == 0 && w == 0) {
          ST128(my, lane16, SM_IDX + r * 1024);
          if (r == 0 && l == 0)
            __builtin_amdgcn_raw_buffer_store_b64((v2u){ep_lo + 1u, ep_hi + (ep_lo == 0xffffffffu ? 1u : 0u)}, rws, 0, FL_E, 16);
        }
        if (__all(max(umax4(aa[0]), umax4(aa[1])) != SENT)) break;
        SLEEP(2);
      }
      STAMP(1 + r, 11);
      const float mb = __uint_as_float(my[1]), ri = __uint_as_float(my[2]);
      f32x4 ag = {0, 0, 0, 0}, au = {0, 0, 0, 0};
#pragma unroll
      for (int i = 0; i < 2; ++i) {
        v4u y;
#pragma unroll
        for (int e = 0; e < 4; ++e) y[e] = norm2(unp(pack2(unp(xa[i][e]) + unp(aa[i][e]))), mb, ri);
        ag = mfma(ffw[2 * i], y, ag);
        au = mfma(ffw[2 * i + 1], y, au);
      }
      f32x4 LAS* red = (f32x4 LAS*)(sm + L_SH);
      if (tok < 8) { red[w * 64 + g4 * 8 + tok] = ag; red[w * 64 + 32 + g4 * 8 + tok] = au; }
      BAR();
      if (w == 0) {
        const f32x2 LAS* rd = (const f32x2 LAS*)red + l;
        f32x2 gt = rd[0], up = rd[64];
#pragma unroll
        for (int i = 1; i < 8; ++i) { gt += rd[i * 128]; up += rd[i * 128 + 64]; }
        const f32x2 gv = unp(pack2(gt)), uv = unp(pack2(up));
        f32x2 sg;
#pragma unroll
        for (int e = 0; e < 2; ++e) sg[e] = gv[e] * __builtin_amdgcn_rcpf(1.0f + __expf(-gv[e]));
        const unsigned o = pack2(unp(pack2(sg)) * uv);
        __builtin_amdgcn_raw_buffer_store_b32(o, rws, gu_u * 256 + (l >> 5) * 128 + ((l >> 1) & 7) * 16 + ((l >> 4) & 1) * 8 + (l & 1) * 4,
                                              SM_HF + r * 32768, 16);
      }
      BAR();
    }
    STAMP(1 + r, 12);
    if (r == 0) qkv_gemm<1>(sm, rws, w0, tag_lo, tag_hi, Wg, Wu, Wd);
    STAMP(1 + r, 13);
    v4u srec;
    {
      IDS
      v4u LAS* scr = (v4u LAS*)(sm + L_SH + 32000);
      if (w == 7) {
        v4u f;
        _Pragma("unroll 1") for (unsigned spins = 0; spins < SPIN; ++spins) {
          f = LD128(xcd * 128 + (l & 7) * 16, SM_IDX + r * 1024);
          if (__all(valid4(f))) break;
          SLEEP(4);
        }
        if (l < 8) scr[l] = f;
      }
      BAR();
      srec = scr[l & 7];
    }
    const int stok = (0xfffe - (int)(srec[0] & 0xffffu)) & 2047;
    if (role == 2) {
      IDS
      v4u ffw[7];
#pragma unroll
      for (int i = 0; i < 7; ++i) ffw[i] = LD128(lane16, OFF_DNF + (dn_u * 56 + 7 * w + i) * 1024);
      v2u xa, aa;
      const unsigned ro = (unsigned)(stok * 1024 + dn_u * 32 + 8 * g4);
      _Pragma("unroll 1") for (unsigned spins = 0; spins < SPIN; ++spins) {
        xa = r == 0 ? *(const v2u*)((const char*)X + ro) : __builtin_amdgcn_raw_buffer_load_b64(rws, ro, OFF_X2, 16);
        aa = __builtin_amdgcn_raw_buffer_load_b64(rws, ro, OFF_AA + r * (2u << 20), 16);
        if (__all(max(aa[0], aa[1]) != SENT)) break;
        SLEEP(2);
      }
      v4u hf[7];
      _Pragma("unroll 1") for (unsigned spins = 0; spins < SPIN; ++spins) {
        unsigned mx = 0;
#pragma unroll
        for (int s = 0; s < 7; ++s) {
          hf[s] = LD128((7 * w + s) * 512 + g4 * 128 + (l & 7) * 16, SM_HF + r * 32768);
          mx = max(mx, umax4(hf[s]));
        }
        if (__all(mx != SENT)) break;
        SLEEP(3);
      }
      STAMP(1 + r, 14);
      f32x4 acc = {0, 0, 0, 0};
#pragma unroll
      for (int s = 0; s < 7; ++s) acc = mfma(ffw[s], hf[s], acc);
      f32x4 LAS* red = (f32x4 LAS*)(sm + L_OS + 16384);
      red[w * 64 + l] = acc;
      BAR();
      if (w == 0 && tok < 8) {
        f32x4 y = red[l];
#pragma unroll
        for (int i = 1; i < 8; ++i) y += red[i * 64 + l];
        unsigned u = srec[0] >> 16;
        u = (u & 0x8000u) ? (u ^ 0x8000u) : (u ^ 0xffffu);
        const float rr = __uint_as_float(u << 16);
        const float gt = rbf(__builtin_amdgcn_rcpf(1.0f + __expf(-rr)));
        const float mb = __uint_as_float(srec[1]), ri = __uint_as_float(srec[2]);
        v2u o;
#pragma unroll
        for (int e = 0; e < 2; ++e) {
          const f32x2 x1 = unp(pack2(unp(xa[e]) + unp(aa[e])));
          const f32x2 y1 = unp(norm2(x1, mb, ri));
          const f32x2 up = unp(pack2(unp(pack2(y[2 * e], y[2 * e + 1])) * gt));
          o[e] = pack2(x1 + unp(pack2(y1 + up)));
        }
        if (r == 0) __builtin_amdgcn_raw_buffer_store_b64(o, rws, tok * 1024 + dn_u * 32 + 8 * g4, SM_X2S, 16);
        else *(v2u*)((char*)Out + ro) = o;
      }
      BAR();
    }
    {
      IDS
      const unsigned long long own = __ballot(stok == myrow);
      if (r == 0) {
        const int lo = 128 * t - 16;
        const bool inr = stok >= lo && stok < lo + 144;
        const unsigned inmask = (unsigned)__ballot(inr) & 0xffu;
        if (inmask) {
          if ((inmask >> w) & 1) {
            v4u xv;
            _Pragma("unroll 1") for (unsigned spins = 0; spins < SPIN; ++spins) {
              xv = LD128(lane16, SM_X2S + w * 1024);
              if (__all(valid4(xv))) break;
              SLEEP(3);
            }
            f32x2 x[4] = {unp(xv[0]), unp(xv[1]), unp(xv[2]), unp(xv[3])};
            float mb, ri;
            *(v4u LAS*)(sm + L_OS + l * 128 + w * 16) = ln_row(x, mb, ri);
          }
          BAR();
          if (w < 6) {
            f32x4 acc = {0, 0, 0, 0};
#pragma unroll
            for (int s = 0; s < 16; ++s)
              acc = mfma(*(const v4u LAS*)(sm + L_W + (6 * s + w) * 1024 + lane16),
                         *(const v4u LAS*)(sm + L_OS + (4 * s + g4) * 128 + (l & 7) * 16), acc);
            if (inr && tok < 8)
              *(v2u LAS*)(sm + L_SH + (2 * w + (g4 >> 1)) * PLANE + 8 * (g4 & 1) + (stok - lo) * 16) = packacc(acc);
          }
          if (own) {
            const int slot = __builtin_ctzll(own);
            _Pragma("unroll 1") for (unsigned spins = 0; spins < SPIN; ++spins) {
              xres = LD128(lane16, SM_X2S + slot * 1024);
              if (__all(valid4(xres))) break;
              SLEEP(2);
            }
            ST128(xres, lane16, OFF_X2 + myrow * 1024);
          }
          BAR();
        }
        STAMP(2, 2);
        attention(sm, rws, w0, 1);
      } else if (!own) {
        *(v4u*)((char*)Out + myrow * 1024 + lane16) = xo;
      }
    }
    STAMP(1 + r, 15);
  }
}

extern "C" void kernel_launch(void* const* d_in, const int* in_sizes, int n_in, void* d_out, int out_size,
                              void* d_ws, size_t ws_size, hipStream_t stream) {
  (void)hipFuncSetAttribute((const void*)k_main, hipFuncAttributeMaxDynamicSharedMemorySize, L_TOTAL);
  k_main<<<256, 512, L_TOTAL, stream>>>((const u16*)d_in[0], (const u16*)d_in[1], (const u16*)d_in[2], (const u16*)d_in[4],
                                        (const u16*)d_in[5], (const u16*)d_in[6], (const u16*)d_in[7], (u16*)d_out, (char*)d_ws);
}
```

```cpp
#include <hip/hip_runtime.h>
#include <hip/hip_bf16.h>
#include <stdint.h>

#define LAS __attribute__((address_space(3)))
typedef unsigned short u16;
typedef short v4s __attribute__((ext_vector_type(4)));
typedef short v8s __attribute__((ext_vector_type(8)));
typedef __bf16 v2bf __attribute__((ext_vector_type(2)));
typedef float f32x4 __attribute__((ext_vector_type(4)));
typedef float f32x2 __attribute__((ext_vector_type(2)));
typedef float f32x16 __attribute__((ext_vector_type(16)));
typedef unsigned v4u __attribute__((ext_vector_type(4)));
typedef unsigned v2u __attribute__((ext_vector_type(2)));

#define STAMP(k, i)
#define TS(v)
#define TSPUT(k, i, v)
#define WPUT(i, v)
#define WST(i)

constexpr int S = 2048, D = 512, F = 1792;
constexpr unsigned SENT = 0xFFFFFFFFu;
constexpr unsigned SPIN = 1u << 18;
constexpr unsigned OFF_Z1 = 0, OFF_Z2 = 2u << 20, OFF_OF = 4u << 20, OFF_AA = 8u << 20, OFF_X2 = 12u << 20,
                   OFF_SM = 14u << 20, OFF_WF = 16u << 20, OFF_WOF = 18u << 20, OFF_GUF = 19u << 20,
                   OFF_DNF = 23u << 20, WS_BYTES = 25u << 20;
constexpr unsigned SM_REC = OFF_SM, SM_HF = OFF_SM + 65536, SM_X2S = OFF_SM + 131072, SM_IDX = OFF_SM + 139264;
constexpr unsigned FL_A = OFF_SM + 262144;
constexpr unsigned FL_B = FL_A + 65536;
constexpr unsigned FL_C = FL_B + 4096;
constexpr unsigned FL_E = FL_C + 4096;
constexpr int L_W = 0, L_SH = 98304, L_OS = 131072, L_TOTAL = 163840;
constexpr int PLANE = 2304;
constexpr unsigned CODE_BYTES = 28672;

__device__ __forceinline__ float bf_lo(unsigned u) { return __uint_as_float(u << 16); }
__device__ __forceinline__ float bf_hi(unsigned u) { return __uint_as_float(u & 0xffff0000u); }
__device__ __forceinline__ unsigned pack2(f32x2 f) {
  v2bf c = __builtin_convertvector(f, v2bf);
  return __builtin_bit_cast(unsigned, c);
}
__device__ __forceinline__ unsigned pack2(float a, float b) { return pack2((f32x2){a, b}); }
__device__ __forceinline__ f32x2 unp(unsigned u) { return (f32x2){bf_lo(u), bf_hi(u)}; }
__device__ __forceinline__ float rbf(float a) {
  __bf16 c = (__bf16)a;
  return __uint_as_float(((unsigned)__builtin_bit_cast(u16, c)) << 16);
}
__device__ __forceinline__ float dot2(unsigned a, unsigned b, float c) {
  return __builtin_amdgcn_fdot2_f32_bf16(__builtin_bit_cast(v2bf, a), __builtin_bit_cast(v2bf, b), c, false);
}
__device__ __forceinline__ v4u tr2(const char LAS* p, int second) {
  const v4s a = __builtin_amdgcn_ds_read_tr16_b64_v4i16((LAS v4s*)p);
  const v4s b = __builtin_amdgcn_ds_read_tr16_b64_v4i16((LAS v4s*)(p + second));
  const v8s r = {a[0], a[1], a[2], a[3], b[0], b[1], b[2], b[3]};
  return __builtin_bit_cast(v4u, r);
}
__device__ __forceinline__ f32x4 mfma(v4u a, v4u b, f32x4 c) {
  return __builtin_amdgcn_mfma_f32_16x16x32_bf16(__builtin_bit_cast(v8s, a), __builtin_bit_cast(v8s, b), c, 0, 0, 0);
}
__device__ __forceinline__ v2u packacc(f32x4 a) { return (v2u){pack2(a[0], a[1]), pack2(a[2], a[3])}; }
typedef __amdgpu_buffer_rsrc_t srd_t;
__device__ __forceinline__ srd_t make_srd(const void* p, unsigned bytes) {
  return __builtin_amdgcn_make_buffer_rsrc((void*)p, 0, bytes, 0x00020000);
}
__device__ __forceinline__ void dma16(srd_t srd, unsigned voff, unsigned soff, unsigned lds) {
  unsigned keep;
  asm volatile("s_mov_b32 %0, m0\n\ts_mov_b32 m0, %3\n\ts_nop 0\n\tbuffer_load_dwordx4 %1, %2, %4 offen lds\n\ts_mov_b32 m0, %0"
               : "=&s"(keep) : "v"(voff), "s"(srd), "s"(lds), "s"(soff));
}
__device__ __forceinline__ void dma16_sc1(srd_t srd, unsigned voff, unsigned soff, unsigned lds) {
  unsigned keep;
  asm volatile("s_mov_b32 %0, m0\n\ts_mov_b32 m0, %3\n\ts_nop 0\n\tbuffer_load_dwordx4 %1, %2, %4 offen sc1 lds\n\ts_mov_b32 m0, %0"
               : "=&s"(keep) : "v"(voff), "s"(srd), "s"(lds), "s"(soff));
}
__device__ __forceinline__ void touch_sc1(srd_t srd, unsigned voff, unsigned soff, unsigned lds) {
  unsigned keep;
  asm volatile("s_mov_b32 %0, m0\n\ts_mov_b32 m0, %3\n\ts_nop 0\n\tbuffer_load_dword %1, %2, %4 offen sc1 lds\n\ts_mov_b32 m0, %0"
               : "=&s"(keep) : "v"(voff), "s"(srd), "s"(lds), "s"(soff));
}
__device__ __forceinline__ void aload16(v4u& d, srd_t srd, unsigned voff, unsigned soff) {
  asm volatile("buffer_load_dwordx4 %0, %1, %2, %3 offen" : "=v"(d) : "v"(voff), "s"(srd), "s"(soff));
}
__device__ __forceinline__ void aload16_sc1(v4u& d, srd_t srd, unsigned voff, unsigned soff) {
  asm volatile("buffer_load_dwordx4 %0, %1, %2, %3 offen sc1" : "=v"(d) : "v"(voff), "s"(srd), "s"(soff));
}
__device__ __forceinline__ void astore16_sc1(v4u d, srd_t srd, unsigned voff, unsigned soff) {
  asm volatile("buffer_store_dwordx4 %0, %1, %2, %3 offen sc1\n\ts_nop 1" :: "v"(d), "v"(voff), "s"(srd), "s"(soff));
}
#define SLEEP(n) do { __builtin_amdgcn_s_sleep(n); asm volatile("" ::: "memory"); } while (0)
#define VMWAIT(n) asm volatile("s_waitcnt vmcnt(" #n ")" ::: "memory")
#define BAR() do { asm volatile("s_waitcnt lgkmcnt(0)" ::: "memory"); __builtin_amdgcn_s_barrier(); asm volatile("" ::: "memory"); } while (0)
#define LD128(voff, soff) __builtin_amdgcn_raw_buffer_load_b128(rws, voff, soff, 16)
__device__ __forceinline__ void st128(v4u d, srd_t srd, unsigned voff, unsigned soff) {
  asm volatile("buffer_store_dwordx4 %0, %1, %2, %3 offen sc1\n\ts_nop 1" :: "v"(d), "v"(voff), "s"(srd), "s"(soff) : "memory");
}
#define ST128(v, voff, soff) st128(v, rws, voff, soff)
__device__ __forceinline__ unsigned umax4(v4u v) { return max(max(v[0], v[1]), max(v[2], v[3])); }
__device__ __forceinline__ bool valid4(v4u v) { return umax4(v) != SENT; }

template <int CTRL, int RMASK>
__device__ __forceinline__ float dpp_add(float v) {
  int t = __builtin_amdgcn_update_dpp(0, __float_as_int(v), CTRL, RMASK, 0xf, true);
  return v + __int_as_float(t);
}
__device__ __forceinline__ float wave_sum(float v) {
  v = dpp_add<0x111, 0xf>(v); v = dpp_add<0x112, 0xf>(v); v = dpp_add<0x114, 0xf>(v); v = dpp_add<0x118, 0xf>(v);
  v = dpp_add<0x142, 0xa>(v); v = dpp_add<0x143, 0xc>(v);
  return __int_as_float(__builtin_amdgcn_readlane(__float_as_int(v), 63));
}
template <int CTRL, int RMASK>
__device__ __forceinline__ unsigned dpp_max(unsigned v) {
  unsigned t = (unsigned)__builtin_amdgcn_update_dpp(0, (int)v, CTRL, RMASK, 0xf, true);
  return v > t ? v : t;
}
__device__ __forceinline__ unsigned wave_max(unsigned v) {
  v = dpp_max<0x111, 0xf>(v); v = dpp_max<0x112, 0xf>(v); v = dpp_max<0x114, 0xf>(v); v = dpp_max<0x118, 0xf>(v);
  v = dpp_max<0x142, 0xa>(v); v = dpp_max<0x143, 0xc>(v);
  return (unsigned)__builtin_amdgcn_readlane((int)v, 63);
}
__device__ __forceinline__ unsigned norm2(f32x2 x, float mb, float ri) { return pack2(unp(pack2(x - mb)) * ri); }
__device__ __forceinline__ v4u ln_row(const f32x2 (&x)[4], float& mb, float& ri) {
  const float c = __int_as_float(__builtin_amdgcn_readfirstlane(__float_as_int(x[0][0])));
  const f32x2 d0 = x[0] - c, d1 = x[1] - c, d2 = x[2] - c, d3 = x[3] - c;
  const f32x2 s2 = (d0 + d1) + (d2 + d3);
  const f32x2 q2 = (d0 * d0 + d1 * d1) + (d2 * d2 + d3 * d3);
  float s = s2[0] + s2[1], q = q2[0] + q2[1];
  s = dpp_add<0x111, 0xf>(s); q = dpp_add<0x111, 0xf>(q);
  s = dpp_add<0x112, 0xf>(s); q = dpp_add<0x112, 0xf>(q);
  s = dpp_add<0x114, 0xf>(s); q = dpp_add<0x114, 0xf>(q);
  s = dpp_add<0x118, 0xf>(s); q = dpp_add<0x118, 0xf>(q);
  s = dpp_add<0x142, 0xa>(s); q = dpp_add<0x142, 0xa>(q);
  s = dpp_add<0x143, 0xc>(s); q = dpp_add<0x143, 0xc>(q);
  const float dm = __int_as_float(__builtin_amdgcn_readlane(__float_as_int(s), 63)) * (1.0f / D);
  const float var = fmaxf(__int_as_float(__builtin_amdgcn_readlane(__float_as_int(q), 63)) * (1.0f / D) - dm * dm, 0.f);
  mb = rbf(c + dm);
  const float sg = rbf(__builtin_amdgcn_sqrtf(rbf(rbf(var) + 1e-5f)));
  ri = __builtin_amdgcn_rcpf(sg);
  return (v4u){norm2(x[0], mb, ri), norm2(x[1], mb, ri), norm2(x[2], mb, ri), norm2(x[3], mb, ri)};
}

#define IDS                                                                                              \
  int tid = threadIdx.x, w = w0, b = blockIdx.x;                                                         \
  asm volatile("" : "+v"(tid)); asm volatile("" : "+s"(w)); asm volatile("" : "+s"(b));                  \
  const int l = tid & 63, xcd = b & 7, jj = b >> 3;                                                      \
  const int t = 4 * (xcd >> 1) + (jj >> 3), j = 8 * (xcd & 1) + (jj & 7);                                \
  const int g4 = l >> 4, tok = l & 15;                                                                   \
  const int rq = j >> 2, cq = j & 3, myrow = 128 * t + 8 * j + w;                                        \
  const int gu_u = 14 * xcd + jj, dn_u = 4 * xcd + (jj - 14);                                            \
  const unsigned lane16 = l * 16;                                                                        \
  const int rth = t > 0 ? 8 * t - 1 : 0;                                                                 \
  (void)rq; (void)cq; (void)myrow; (void)gu_u; (void)dn_u; (void)lane16; (void)rth; (void)tok; (void)g4;

__device__ __forceinline__ void attention(char LAS* sm, srd_t rws, int w0, int r) {
  IDS
  const char LAS* rp = sm + L_SH + g4 * PLANE + (16 + 16 * w + tok) * 16;
  const v4u q0 = *(const v4u LAS*)rp;
  float d[8];
#pragma unroll
  for (int k = 0; k < 8; ++k) {
    const v4u k0 = *(const v4u LAS*)(rp + 4 * PLANE - 32 * k);
    d[k] = dot2(q0[3], k0[3], dot2(q0[2], k0[2], dot2(q0[1], k0[1], dot2(q0[0], k0[0], 0.f))));
  }
  float lg[4];
#pragma unroll
  for (int k = 0; k < 4; ++k) {
    const auto sw = __builtin_amdgcn_permlane16_swap(__float_as_uint(d[k]), __float_as_uint(d[k + 4]), false, false);
    lg[k] = rbf(__uint_as_float(sw[0]) + __uint_as_float(sw[1]));
  }
  if (t == 0) {
#pragma unroll
    for (int k = 0; k < 4; ++k) lg[k] = (16 * w + tok - 2 * k - 8 * (g4 & 1) >= 0) ? lg[k] : -4e9f;
  }
  float m = fmaxf(fmaxf(lg[0], lg[1]), fmaxf(lg[2], lg[3]));
  {
    const auto sw = __builtin_amdgcn_permlane16_swap(__float_as_uint(m), __float_as_uint(m), false, false);
    m = fmaxf(__uint_as_float(sw[0]), __uint_as_float(sw[1]));
  }
  constexpr float C = 0.25f * 1.44269504088896341f;
#pragma unroll
  for (int k = 0; k < 4; ++k) lg[k] = __builtin_amdgcn_exp2f((lg[k] - m) * C);
  float sum = (lg[0] + lg[1]) + (lg[2] + lg[3]);
  {
    const auto sw = __builtin_amdgcn_permlane16_swap(__float_as_uint(sum), __float_as_uint(sum), false, false);
    sum = __uint_as_float(sw[0]) + __uint_as_float(sw[1]);
  }
  const float inv = __builtin_amdgcn_rcpf(sum);
  const unsigned pa = pack2(lg[0] * inv, lg[1] * inv), pb = pack2(lg[2] * inv, lg[3] * inv);
  const auto sa = __builtin_amdgcn_permlane16_swap(pa, pa, false, false);
  const auto sb = __builtin_amdgcn_permlane16_swap(pb, pb, false, false);
  const unsigned pp[4] = {sa[0], sb[0], sa[1], sb[1]};
  float o[8];
#pragma unroll
  for (int e = 0; e < 8; ++e) o[e] = 0.f;
#pragma unroll
  for (int k = 0; k < 4; ++k) {
    const v4u v0 = *(const v4u LAS*)(rp + 8 * PLANE - 64 * k), v1 = *(const v4u LAS*)(rp + 8 * PLANE - 64 * k - 32);
#pragma unroll
    for (int e = 0; e < 4; ++e) {
      o[2 * e] = dot2(pp[k], __builtin_amdgcn_perm(v1[e], v0[e], 0x05040100u), o[2 * e]);
      o[2 * e + 1] = dot2(pp[k], __builtin_amdgcn_perm(v1[e], v0[e], 0x07060302u), o[2 * e + 1]);
    }
  }
  const v4u ov = {pack2(o[0], o[1]), pack2(o[2], o[3]), pack2(o[4], o[5]), pack2(o[6], o[7])};
  ST128(ov, lane16, OFF_OF + r * (2u << 20) + ((8 * t + w) * 16 + j) * 1024);
}

struct Unit { srd_t srd; unsigned stride, dst, dstep, dodd; };
__device__ __forceinline__ Unit ffn_unit(int u, const u16* Wg, const u16* Wu, const u16* Wd) {
  Unit r;
  const char* src = (const char*)Wd;
  r.stride = 1024; r.dst = WS_BYTES; r.dstep = 0; r.dodd = 0;
  if (u < 896) {
    const int up = u >= 448, v = u - 448 * up, s = v / 28, nb = v % 28;
    src = (const char*)(up ? Wu : Wg) + (size_t)(32 * s) * (F * 2) + nb * 128; r.stride = F * 2;
    r.dst = OFF_GUF + ((4 * nb * 16 + s) * 2 + up) * 1024; r.dstep = 2 * 32 * 1024; r.dodd = 32 * 1024;
  } else if (u < 1344) {
    const int s = (u - 896) >> 3, nb = u & 7;
    src = (const char*)Wd + (size_t)(32 * s) * 1024 + nb * 128;
    r.dst = OFF_DNF + (4 * nb * 56 + s) * 1024; r.dstep = 2 * 56 * 1024; r.dodd = 56 * 1024;
  }
  r.srd = make_srd(src, u < 1344 ? 32 * F * 2 : 0);
  return r;
}
__device__ __forceinline__ void unit_store(char LAS* scr, srd_t rws, int l, const v4u (&wv)[4], unsigned dst, unsigned dstep, unsigned dodd) {
#pragma unroll
  for (int i = 0; i < 4; ++i) *(v4u LAS*)(scr + i * 1024 + l * 16) = wv[i];
  asm volatile("s_waitcnt lgkmcnt(0)" ::: "memory");
  const char LAS* p = scr + (8 * (l >> 4) + ((l & 15) >> 2)) * 128 + 8 * (l & 3);
  v4s a[4], c[4];
#pragma unroll
  for (int i = 0; i < 4; ++i) {
    a[i] = __builtin_amdgcn_ds_read_tr16_b64_v4i16((LAS v4s*)(p + 32 * i));
    c[i] = __builtin_amdgcn_ds_read_tr16_b64_v4i16((LAS v4s*)(p + 32 * i + 512));
  }
#pragma unroll
  for (int i = 0; i < 4; ++i) {
    const v8s f = {a[i][0], a[i][1], a[i][2], a[i][3], c[i][0], c[i][1], c[i][2], c[i][3]};
    astore16_sc1(__builtin_bit_cast(v4u, f), rws, l * 16, dst + (i >> 1) * dstep + (i & 1) * dodd);
  }
  asm volatile("" ::: "memory");
}
__device__ __forceinline__ void wait_all_flags(srd_t rws, int l, unsigned off, unsigned lo, unsigned hi) {
  _Pragma("unroll 1") for (unsigned spins = 0; spins < SPIN; ++spins) {
    const v4u a = LD128(l * 32, off), c = LD128(l * 32 + 16, off);
    if (__all((((a[0] ^ lo) | (a[1] ^ hi)) | ((a[2] ^ lo) | (a[3] ^ hi)) | ((c[0] ^ lo) | (c[1] ^ hi)) | ((c[2] ^ lo) | (c[3] ^ hi))) == 0u)) break;
    SLEEP(3);
  }
}
constexpr int L_CTL = L_SH + 32704;
#define WFRAG(s, ct) ((6 * (s) + (ct)) * 1024)

#define WAIT_TIE4(n, a, i) do { VMWAIT(n); __builtin_amdgcn_sched_barrier(0); asm volatile("" : "+v"(a[i]), "+v"(a[i + 1]), "+v"(a[i + 2]), "+v"(a[i + 3])); } while (0)
template <int R>
__device__ __forceinline__ void qkv_gemm(char LAS* sm, srd_t rws, int w0, unsigned tag_lo, unsigned tag_hi,
                                         const u16* Wg, const u16* Wu, const u16* Wd) {
  IDS
  if (R == 1 && tid == 64) *(unsigned LAS*)(sm + L_CTL) = 0u;
  if (w == 0) {
    int fi = l < 16 ? 16 * t + l : (t > 0 ? 16 * t - 2 + (l & 1) : 0);
    if (R == 0 && l >= 32) fi = 16 * ((l >> 1) & 15) + (j & 14) + (l & 1);
    _Pragma("unroll 1") for (unsigned spins = 0; spins < SPIN; ++spins) {
      const v2u f = __builtin_amdgcn_raw_buffer_load_b64(rws, fi * 128, FL_A + R * 32768, 16);
      if (__all(((f[0] ^ (tag_lo + R)) | (f[1] ^ tag_hi)) == 0u)) break;
      SLEEP(3);
    }
  }
  BAR();
  STAMP(3, 4 + 4 * R);
  unsigned long long ta, tb, tc, td;
  TS(ta);
  const unsigned zo = R ? OFF_Z2 : OFF_Z1;
  if (w < 4) {
    const unsigned lo = (unsigned)(((l >> 4) & 1) * 16384 + (l >> 5) * 256 + tok * 16);
    const unsigned zb = zo + (8 * t + 2 * w) * 16384;
    v4u z[32];
#pragma unroll
    for (int u = 0; u < 8; ++u) aload16_sc1(z[u], rws, lo, zb + u * 512);
    const char LAS* wp = sm + L_W + ((l >> 4) & 1) * 1024 + (l >> 5) * 256 + tok * 16;
    f32x16 acc[3];
#pragma unroll
    for (int c = 0; c < 3; ++c)
#pragma unroll
      for (int e = 0; e < 16; ++e) acc[c][e] = 0.f;
    v4u f[3][3];
    TS(tb);
    if (R == 0) BAR();
    TS(tc);
#pragma unroll
    for (int u = 8; u < 12; ++u) aload16_sc1(z[u], rws, lo, zb + u * 512);
#pragma unroll
    for (int u = 0; u < 2; ++u)
#pragma unroll
      for (int c = 0; c < 3; ++c) f[u][c] = *(const v4u LAS*)(wp + (6 * (u >> 1) + 2 * c) * 1024 + (u & 1) * 512);
#pragma unroll
    for (int u = 0; u < 32; ++u) {
      if (!(u & 3)) { if (u < 24) WAIT_TIE4(8, z, u); else if (u == 24) WAIT_TIE4(4, z, 24); else WAIT_TIE4(0, z, 28); }
      if (R == 0 && (u & 7) == 6 && u < 30) { asm volatile("" ::: "memory"); __builtin_amdgcn_s_barrier(); asm volatile("" ::: "memory"); }
      if (u < 30) {
#pragma unroll
        for (int c = 0; c < 3; ++c)
          f[(u + 2) % 3][c] = *(const v4u LAS*)(wp + (6 * ((u + 2) >> 1) + 2 * c) * 1024 + (u & 1) * 512);
      }
      __builtin_amdgcn_sched_barrier(0);
#pragma unroll
      for (int c = 0; c < 3; ++c)
        acc[c] = __builtin_amdgcn_mfma_f32_32x32x16_bf16(__builtin_bit_cast(v8s, f[u % 3][c]), __builtin_bit_cast(v8s, z[u]), acc[c], 0, 0, 0);
      if (u < 20) aload16_sc1(z[u + 12], rws, lo, zb + (u + 12) * 512);
      __builtin_amdgcn_sched_barrier(0);
    }
    TS(td);
    WPUT(4 * R, ta); WPUT(4 * R + 1, tb); WPUT(4 * R + 2, tc); WPUT(4 * R + 3, td);
    BAR();
    char LAS* qp = sm + L_SH + (16 + 32 * w + (l & 31)) * 16 + 8 * (l >> 5);
#pragma unroll
    for (int c = 0; c < 3; ++c)
#pragma unroll
      for (int g = 0; g < 4; ++g)
        *(v2u LAS*)(qp + (4 * c + g) * PLANE) =
            (v2u){pack2(acc[c][4 * g], acc[c][4 * g + 1]), pack2(acc[c][4 * g + 2], acc[c][4 * g + 3])};
  } else {
    const int h = w - 4;
    v4u zh[4];
#pragma unroll
    for (int i = 0; i < 4; ++i) aload16_sc1(zh[i], rws, lane16, zo + (rth * 16 + 4 * h + i) * 1024);
    f32x4 acch = {0, 0, 0, 0};
    const char LAS* wh = sm + L_W + (2 + h) * 1024 + lane16;
    const char LAS* hp = sm + L_SH + lane16;
    if (R == 0) {
      const unsigned wb = OFF_WF + j * 98304 + 6 * h * 1024;
      v4u fv[2][4];
      const Unit u0 = ffn_unit(h * 256 + b, Wg, Wu, Wd), u1 = ffn_unit(1024 + h * 256 + b, Wg, Wu, Wd);
#pragma unroll
      for (int q = 0; q < 4; ++q) {
        if (q == 0) {
#pragma unroll
          for (int p = 0; p < 6; ++p) dma16_sc1(rws, lane16, wb + p * 1024, L_W + (6 * h + p) * 1024);
        }
        if (q < 3) {
#pragma unroll
          for (int p = 0; p < 6; ++p) dma16_sc1(rws, lane16, wb + (24 * (q + 1) + p) * 1024, L_W + (24 * (q + 1) + 6 * h + p) * 1024);
        }
        if (q == 2) {
#pragma unroll
          for (int i = 0; i < 4; ++i) aload16(fv[0][i], u0.srd, (unsigned)(l >> 3) * u0.stride + (l & 7) * 16, 8 * i * u0.stride);
#pragma unroll
          for (int i = 0; i < 4; ++i) aload16(fv[1][i], u1.srd, (unsigned)(l >> 3) * u1.stride + (l & 7) * 16, 8 * i * u1.stride);
        }
        if (q == 0) {
          WAIT_TIE4(6, zh, 0);
#pragma unroll
          for (int i = 0; i < 4; ++i) *(v4u LAS*)(sm + L_SH + (4 * h + i) * 1024 + lane16) = zh[i];
          TS(tb);
        } else if (q == 1) VMWAIT(6);
        else if (q == 2) VMWAIT(14);
        else VMWAIT(8);
        BAR();
        if (q == 0) {
          TS(tc);
          if (tid == 448) __builtin_amdgcn_raw_buffer_store_b64((v2u){tag_lo + 2, tag_hi}, rws, b * 8, FL_B, 16);
        }
      }
#pragma unroll
      for (int s = 0; s < 16; ++s) acch = mfma(*(const v4u LAS*)(wh + s * 6144), *(const v4u LAS*)(hp + s * 1024), acch);
      WAIT_TIE4(4, fv[0], 0);
      unit_store(sm + L_SH + 16384 + h * 4096, rws, l, fv[0], u0.dst, u0.dstep, u0.dodd);
      WAIT_TIE4(4, fv[1], 0);
      unit_store(sm + L_SH + 16384 + h * 4096, rws, l, fv[1], u1.dst, u1.dstep, u1.dodd);
    } else {
      WAIT_TIE4(0, zh, 0);
#pragma unroll
      for (int i = 0; i < 4; ++i) *(v4u LAS*)(sm + L_SH + (4 * h + i) * 1024 + lane16) = zh[i];
      TS(tb);
      asm volatile("s_waitcnt lgkmcnt(0)" ::: "memory");
      if (l == 0) __hip_atomic_fetch_add((unsigned LAS*)(sm + L_CTL), 1u, __ATOMIC_RELAXED, __HIP_MEMORY_SCOPE_WORKGROUP);
      volatile unsigned LAS* ctl = (volatile unsigned LAS*)(sm + L_CTL);
      _Pragma("unroll 1") for (unsigned spins = 0; spins < (SPIN << 4) && *ctl < 4u; ++spins) SLEEP(1);
      asm volatile("" ::: "memory");
      TS(tc);
#pragma unroll
      for (int s = 0; s < 16; ++s) acch = mfma(*(const v4u LAS*)(wh + s * 6144), *(const v4u LAS*)(hp + s * 1024), acch);
    }
    TS(td);
    WPUT(4 * R, ta); WPUT(4 * R + 1, tb); WPUT(4 * R + 2, tc); WPUT(4 * R + 3, td);
    BAR();
    *(v2u LAS*)(sm + L_SH + (2 * (2 + h) + (g4 >> 1)) * PLANE + 8 * (g4 & 1) + tok * 16) = packacc(acch);
  }
}

__global__ __launch_bounds__(512, 2) void k_main(const u16* __restrict__ X, const u16* __restrict__ Wqkv,
                                                 const u16* __restrict__ Wo, const u16* __restrict__ Wr,
                                                 const u16* __restrict__ Wg, const u16* __restrict__ Wu,
                                                 const u16* __restrict__ Wd, u16* __restrict__ Out,
                                                 char* __restrict__ ws) {
  extern __shared__ __attribute__((aligned(16))) char smem[];
  char LAS* sm = (char LAS*)smem;
  asm volatile("" :: "s"(X), "s"(Wqkv), "s"(Wo), "s"(Wr), "s"(Wg), "s"(Wu), "s"(Wd), "s"(Out), "s"(ws));
  const int w0 = __builtin_amdgcn_readfirstlane(threadIdx.x >> 6);
  const srd_t rws = make_srd(ws, WS_BYTES);
  const int role = (blockIdx.x >> 3) < 14 ? 1 : (blockIdx.x >> 3) < 18 ? 2 : 0;
  STAMP(1, 0);
  if (w0 == 7 && (blockIdx.x >> 3) < 4) {
    unsigned long long pc;
    asm volatile("s_getpc_b64 %0" : "=s"(pc));
    touch_sc1(make_srd((const void*)pc, CODE_BYTES), (threadIdx.x & 63) * 128, (blockIdx.x >> 3) * 8192, L_SH + 32256);
  }
  if (w0 == 6) {
    touch_sc1(rws, (threadIdx.x & 63) * 524288 + 448, 0, L_SH + 32256);
    touch_sc1(make_srd(Out, S * D * 2), (threadIdx.x & 63) * 524288, 0, L_SH + 32256);
  }
  v4u xres, wrv;
  v4u wof[16];
  unsigned tag_lo, tag_hi, ep_lo, ep_hi;
  {
    IDS
    v2u ev;
    v4u wv[4];
    asm volatile("buffer_load_dwordx2 %0, off, %1, %2 sc1" : "=v"(ev) : "s"(rws), "s"(FL_E));
    aload16(wrv, make_srd(Wr, D * 2), lane16, 0);
    aload16(xres, make_srd(X, S * D * 2), lane16, myrow * 1024);
    const bool has_unit = w == 0 || (w == 1 && !(j & 1));
    const int up = w == 0 ? (j & 1) : 2;
    {
      const srd_t su = make_srd((const char*)Wqkv + (size_t)(32 * t) * 3072 + up * 1024 + (j >> 1) * 128, has_unit ? 31 * 3072 + 128 : 0);
#pragma unroll
      for (int i = 0; i < 4; ++i) aload16(wv[i], su, (unsigned)(l >> 3) * 3072 + (l & 7) * 16, 8 * i * 3072);
    }
    const srd_t srd_wo = make_srd(Wo, D * D * 2);
    const unsigned wovoff = (unsigned)(g4 * 1024 + ((tok ^ ((g4 | (((w >> 1) & 1) << 2)) << 1)) * 16));
#pragma unroll
    for (int q = 0; q < 2; ++q)
#pragma unroll
      for (int m = 0; m < 4; ++m)
        dma16(srd_wo, wovoff, (unsigned)((128 * q + 4 * (w + 8 * m)) * 1024 + cq * 256), L_W + q * 32768 + (w + 8 * m) * 1024);
    if (w == 0) {
#pragma unroll
      for (int m = 0; m < 4; ++m) dma16(srd_wo, wovoff, (unsigned)((256 + 32 * m) * 1024 + cq * 256), L_OS + 8 * m * 1024);
      VMWAIT(16);
    } else {
      VMWAIT(12);
    }
    __builtin_amdgcn_sched_barrier(0);
    asm volatile("" : "+v"(ev), "+v"(xres), "+v"(wrv));
    STAMP(3, 0);
    ep_lo = __builtin_amdgcn_readfirstlane(ev[0]);
    ep_hi = __builtin_amdgcn_readfirstlane(ev[1]);
    {
      const unsigned long long e = ((unsigned long long)ep_hi << 32) | ep_lo;
      const unsigned long long tg = (e + 1) * 0x9E3779B97F4A7C15ull;
      tag_lo = (unsigned)tg & ~3u; tag_hi = (unsigned)(tg >> 32);
    }
    {
      f32x2 x[4] = {unp(xres[0]), unp(xres[1]), unp(xres[2]), unp(xres[3])};
      float mb, ri;
      *(v4u LAS*)(sm + L_SH + 8192 + w * 1040 + lane16) = ln_row(x, mb, ri);
    }
    if (w == 0) VMWAIT(12); else VMWAIT(8);
    __builtin_amdgcn_sched_barrier(0);
    asm volatile("" : "+v"(wv[0]), "+v"(wv[1]), "+v"(wv[2]), "+v"(wv[3]));
    BAR();
    if (w < 2) {
      if (has_unit) unit_store(sm + L_SH + w * 4096, rws, l, wv, OFF_WF + (((j & 14) * 16 + t) * 6 + 2 * up) * 1024, 98304, 1024);
    } else if (w >= 4) {
#pragma unroll
      for (int i = 0; i < 2; ++i) {
        const int chunk = 16 * (w - 4) + 8 * i + (l >> 3), r8 = l & 7;
        const v4u v = *(const v4u LAS*)(sm + L_SH + 8192 + r8 * 1040 + chunk * 16);
        ST128(v, (((8 * t + (j >> 1)) * 16 + (chunk >> 2)) * 64 + (chunk & 3) * 16 + 8 * (j & 1) + r8) * 16, OFF_Z1);
      }
    }
    VMWAIT(0);
    STAMP(3, 2);
    BAR();
    if (tid == 0) __builtin_amdgcn_raw_buffer_store_b64((v2u){tag_lo, tag_hi}, rws, (16 * t + j) * 128, FL_A, 16);
    if (w) {
#pragma unroll
      for (int m = 0; m < 4; ++m)
        dma16(srd_wo, wovoff, (unsigned)((256 + 4 * (w + 8 * m)) * 1024 + cq * 256), L_OS + (w + 8 * m) * 1024);
    }
    const char LAS* wp = sm + (8 * g4 + (tok >> 2)) * 256 + ((w ^ ((tok >> 2) | ((g4 & 1) << 2))) * 32) + 8 * (l & 3);
#pragma unroll
    for (int s = 0; s < 8; ++s) wof[s] = tr2(wp + L_W + s * 8192, 1024);
    if (w >= 4) {
      const v4u sv = {SENT, SENT, SENT, SENT};
      const int w2 = 2 * (w - 4);
      const unsigned ao = (unsigned)((128 * t + 32 * rq + 4 * w2 + g4) * 1024 + cq * 256 + tok * 16);
      astore16_sc1(sv, rws, lane16, OFF_OF + ((8 * t + w2) * 16 + j) * 1024);
      astore16_sc1(sv, rws, lane16, OFF_OF + ((8 * t + w2 + 1) * 16 + j) * 1024);
      astore16_sc1(sv, rws, ao, OFF_AA);
      astore16_sc1(sv, rws, ao, OFF_AA + 4096);
      if (w == 4) astore16_sc1(sv, rws, l < 36 ? lane16 : WS_BYTES, OFF_SM + b * 576);
    }
    STAMP(3, 3);
  }
  qkv_gemm<0>(sm, rws, w0, tag_lo, tag_hi, Wg, Wu, Wd);
  {
    IDS
    const char LAS* wp = sm + L_OS + (8 * g4 + (tok >> 2)) * 256 + ((w ^ ((tok >> 2) | ((g4 & 1) << 2))) * 32) + 8 * (l & 3);
#pragma unroll
    for (int s = 8; s < 12; ++s) wof[s] = tr2(wp + (s - 8) * 8192, 1024);
    BAR();
    const srd_t srd_wo = make_srd(Wo, D * D * 2);
    const unsigned wovoff = (unsigned)(g4 * 1024 + ((tok ^ ((g4 | (((w >> 1) & 1) << 2)) << 1)) * 16));
#pragma unroll
    for (int m = 0; m < 4; ++m)
      dma16(srd_wo, wovoff, (unsigned)((384 + 4 * (w + 8 * m)) * 1024 + cq * 256), L_OS + (w + 8 * m) * 1024);
    STAMP(1, 2);
    attention(sm, rws, w0, 0);
    VMWAIT(1);
    BAR();
#pragma unroll
    for (int s = 12; s < 16; ++s) wof[s] = tr2(wp + (s - 12) * 8192, 1024);
    BAR();
  }

#pragma unroll 1
  for (int r = 0; r < 2; ++r) {
    STAMP(1 + r, 3);
    {
      IDS
      v4u of[4];
      const unsigned ofb = OFF_OF + r * (2u << 20) + ((8 * t + 2 * rq) * 16 + 4 * w) * 1024;
      if (r == 0) wait_all_flags(rws, l, FL_B, tag_lo + 2, tag_hi);
      else SLEEP(10);
      _Pragma("unroll 1") for (unsigned spins = 0; spins < SPIN; ++spins) {
#pragma unroll
        for (int i = 0; i < 4; ++i) of[i] = LD128(lane16, ofb + i * 1024);
        if (__all(max(max(umax4(of[0]), umax4(of[1])), max(umax4(of[2]), umax4(of[3]))) != SENT)) break;
        SLEEP(2);
      }
#pragma unroll
      for (int i = 0; i < 4; ++i) *(v4u LAS*)(sm + L_OS + (4 * w + i) * 1024 + lane16) = of[i];
    }
    BAR();
    STAMP(1 + r, 4);
    {
      IDS
      f32x4 a0 = {0, 0, 0, 0}, a1 = {0, 0, 0, 0};
      const char LAS* op = sm + L_OS + lane16;
      v4u f[2][8];
#pragma unroll
      for (int i = 0; i < 4; ++i) { f[0][2 * i] = *(const v4u LAS*)(op + i * 1024); f[0][2 * i + 1] = *(const v4u LAS*)(op + (16 + i) * 1024); }
#pragma unroll
      for (int c = 0; c < 4; ++c) {
        if (c < 3) {
#pragma unroll
          for (int i = 0; i < 4; ++i) {
            f[(c + 1) & 1][2 * i] = *(const v4u LAS*)(op + (4 * c + 4 + i) * 1024);
            f[(c + 1) & 1][2 * i + 1] = *(const v4u LAS*)(op + (20 + 4 * c + i) * 1024);
          }
        }
        __builtin_amdgcn_sched_barrier(0);
#pragma unroll
        for (int i = 0; i < 4; ++i) {
          a0 = mfma(wof[4 * c + i], f[c & 1][2 * i], a0);
          a1 = mfma(wof[4 * c + i], f[c & 1][2 * i + 1], a1);
        }
        __builtin_amdgcn_sched_barrier(0);
      }
      char LAS* tp = sm + L_SH + tok * 272 + 32 * w + 8 * g4;
      *(v2u LAS*)tp = packacc(a0);
      *(v2u LAS*)(tp + 16 * 272) = packacc(a1);
      BAR();
      const v4u v = *(const v4u LAS*)(sm + L_SH + (4 * w + g4) * 272 + tok * 16);
      ST128(v, (128 * t + 32 * rq + 4 * w + g4) * 1024 + cq * 256 + tok * 16, OFF_AA + r * (2u << 20));
    }
    STAMP(1 + r, 5);
    v4u xo;
    {
      IDS
      SLEEP(10);
      v4u av;
      _Pragma("unroll 1") for (unsigned spins = 0; spins < SPIN; ++spins) {
        av = LD128(lane16, OFF_AA + r * (2u << 20) + myrow * 1024);
        if (__all(valid4(av))) break;
        SLEEP(2);
      }
      STAMP(1 + r, 6);
      f32x2 x1[4];
      float mb, ri;
#pragma unroll
      for (int e = 0; e < 4; ++e) x1[e] = unp(pack2(unp(xres[e]) + unp(av[e])));
      const v4u y = ln_row(x1, mb, ri);
      const float d = wave_sum(dot2(y[3], wrv[3], dot2(y[2], wrv[2], dot2(y[1], wrv[1], dot2(y[0], wrv[0], 0.f)))));
      unsigned u = __float_as_uint(rbf(d)) >> 16;
      u = (u & 0x8000u) ? (u ^ 0xffffu) : (u ^ 0x8000u);
      v4u rec = {(u << 16) | (unsigned)(0xfffe - myrow), __float_as_uint(mb), __float_as_uint(ri), 0u};
      rec[1] = rec[1] == SENT ? 0x7fc00000u : rec[1];
      rec[2] = rec[2] == SENT ? 0x7fc00000u : rec[2];
      if (l == 0) *(v4u LAS*)(sm + L_SH + 8960 + w * 16) = rec;
      BAR();
      if (tid < 8) ST128(*(const v4u LAS*)(sm + L_SH + 8960 + tid * 16), (128 * t + 8 * j + tid) * 16, SM_REC + r * 32768);
      STAMP(1 + r, 7);
#pragma unroll
      for (int e = 0; e < 4; ++e) xo[e] = pack2(x1[e] + unp(y[e]));
      if (r == 0) {
        xres = xo;
        ST128(xo, lane16, OFF_X2 + myrow * 1024);
#pragma unroll
        for (int e = 0; e < 4; ++e) x1[e] = unp(xo[e]);
        *(v4u LAS*)(sm + L_SH + 9216 + w * 1040 + lane16) = ln_row(x1, mb, ri);
        BAR();
        const int chunk = 8 * w + (l >> 3), r8 = l & 7;
        const v4u v = *(const v4u LAS*)(sm + L_SH + 9216 + r8 * 1040 + chunk * 16);
        ST128(v, (((8 * t + (j >> 1)) * 16 + (chunk >> 2)) * 64 + (chunk & 3) * 16 + 8 * (j & 1) + r8) * 16, OFF_Z2);
        const v4u sv = {SENT, SENT, SENT, SENT};
        ST128(sv, lane16, OFF_OF + (2u << 20) + ((8 * t + w) * 16 + j) * 1024);
        ST128(sv, (128 * t + 32 * rq + 4 * w + g4) * 1024 + cq * 256 + tok * 16, OFF_AA + (2u << 20));
        if (role != 1) {
          VMWAIT(0);
          BAR();
          if (tid == 0) __builtin_amdgcn_raw_buffer_store_b64((v2u){tag_lo + 1, tag_hi}, rws, (16 * t + j) * 128, FL_A + 32768, 16);
        }
      }
    }
    STAMP(1 + r, 8);
    if (role == 1) {
      IDS
      unsigned LAS* scr = (unsigned LAS*)(sm + L_OS);
      v4u LAS* picks = (v4u LAS*)(sm + L_OS + 1024) + 8 * w;
      v4u LAS* cand = (v4u LAS*)(sm + L_OS + 2048);
      unsigned LAS* ckey = (unsigned LAS*)(sm + L_SH + 17664);
      if (tid == 0) scr[8] = 0u;
      if (tid < 64) ckey[tid] = 0u;
      v4u rc[4];
      _Pragma("unroll 1") for (unsigned spins = 0; spins < SPIN; ++spins) {
#pragma unroll
        for (int e = 0; e < 4; ++e) rc[e] = LD128(tid * 16, SM_REC + r * 32768 + e * 8192);
        const bool done = __all(max(max(umax4(rc[0]), umax4(rc[1])), max(umax4(rc[2]), umax4(rc[3]))) != SENT);
        if (r == 0 && spins == 0) {
          VMWAIT(0);
          BAR();
          if (tid == 0) __builtin_amdgcn_raw_buffer_store_b64((v2u){tag_lo + 1, tag_hi}, rws, (16 * t + j) * 128, FL_A + 32768, 16);
        }
        if (done) break;
        SLEEP(3);
      }
      unsigned m = max(max(rc[0][0], rc[1][0]), max(rc[2][0], rc[3][0]));
      m = wave_max(m);
      if (l == 0) scr[w] = m;
      BAR();
      STAMP(1 + r, 9);
      v4u ffw[4];
#pragma unroll
      for (int i = 0; i < 4; ++i) ffw[i] = LD128(lane16, OFF_GUF + (gu_u * 16 + 2 * w) * 2048 + i * 1024);
      const v4u m0 = *(const v4u LAS*)scr, m1 = *(const v4u LAS*)(scr + 4);
      const unsigned T0 = min(min(min(m0[0], m0[1]), min(m0[2], m0[3])), min(min(m1[0], m1[1]), min(m1[2], m1[3])));
      unsigned long long bm[4];
      int tot = 0;
#pragma unroll
      for (int e = 0; e < 4; ++e) { bm[e] = __ballot(rc[e][0] >= T0); tot += __popcll(bm[e]); }
      unsigned base = 0;
      if (l == 0) base = __hip_atomic_fetch_add(scr + 8, (unsigned)tot, __ATOMIC_RELAXED, __HIP_MEMORY_SCOPE_WORKGROUP);
      base = __builtin_amdgcn_readfirstlane(base);
#pragma unroll
      for (int e = 0; e < 4; ++e) {
        const unsigned pos = base + __builtin_amdgcn_mbcnt_hi((unsigned)(bm[e] >> 32), __builtin_amdgcn_mbcnt_lo((unsigned)bm[e], 0u));
        if (rc[e][0] >= T0) { cand[pos] = rc[e]; ckey[pos] = rc[e][0]; }
        base += __popcll(bm[e]);
      }
      BAR();
      const int n = __builtin_amdgcn_readfirstlane((int)scr[8]);
      if (n <= 64) {
        const v4u mine = cand[l];
        int rank = 0;
        for (int i = 0; i < n; i += 16) {
          v4u kk[4];
#pragma unroll
          for (int e = 0; e < 4; ++e) kk[e] = *(const v4u LAS*)(ckey + i + 4 * e);
#pragma unroll
          for (int e = 0; e < 16; ++e) rank += kk[e >> 2][e & 3] > mine[0] ? 1 : 0;
        }
        if (l < n && rank < 8) picks[rank] = mine;
      } else {
        for (int c0 = 0; c0 < n; c0 += 64) {
          if (c0 + l < n) {
            const v4u mine = cand[c0 + l];
            int rank = 0;
            for (int i = 0; i < n; ++i) rank += ckey[i] > mine[0] ? 1 : 0;
            if (rank < 8) picks[rank] = mine;
          }
        }
      }
      asm volatile("s_waitcnt lgkmcnt(0)" ::: "memory");
      const v4u my = picks[l & 7];
      const int st = (0xfffe - (int)(my[0] & 0xffffu)) & 2047;
      STAMP(1 + r, 10);
      v4u xa[2], aa[2];
      const unsigned ro = (unsigned)(st * 1024 + 128 * w + 16 * g4);
      _Pragma("unroll 1") for (unsigned spins = 0; spins < SPIN; ++spins) {
#pragma unroll
        for (int i = 0; i < 2; ++i) {
          xa[i] = r == 0 ? *(const v4u*)((const char*)X + ro + 64 * i) : LD128(ro + 64 * i, OFF_X2);
          aa[i] = LD128(ro + 64 * i, OFF_AA + r * (2u << 20));
        }
        if (spins == 0 && gu_u == 0 && w == 0) {
          ST128(my, lane16, SM_IDX + r * 1024);
          if (r == 0 && l == 0)
            __builtin_amdgcn_raw_buffer_store_b64((v2u){ep_lo + 1u, ep_hi + (ep_lo == 0xffffffffu ? 1u : 0u)}, rws, 0, FL_E, 16);
        }
        if (__all(max(umax4(aa[0]), umax4(aa[1])) != SENT)) break;
        SLEEP(2);
      }
      STAMP(1 + r, 11);
      const float mb = __uint_as_float(my[1]), ri = __uint_as_float(my[2]);
      f32x4 ag = {0, 0, 0, 0}, au = {0, 0, 0, 0};
#pragma unroll
      for (int i = 0; i < 2; ++i) {
        v4u y;
#pragma unroll
        for (int e = 0; e < 4; ++e) y[e] = norm2(unp(pack2(unp(xa[i][e]) + unp(aa[i][e]))), mb, ri);
        ag = mfma(ffw[2 * i], y, ag);
        au = mfma(ffw[2 * i + 1], y, au);
      }
      f32x4 LAS* red = (f32x4 LAS*)(sm + L_SH);
      if (tok < 8) { red[w * 64 + g4 * 8 + tok] = ag; red[w * 64 + 32 + g4 * 8 + tok] = au; }
      BAR();
      if (w == 0) {
        const f32x2 LAS* rd = (const f32x2 LAS*)red + l;
        f32x2 gt = rd[0], up = rd[64];
#pragma unroll
        for (int i = 1; i < 8; ++i) { gt += rd[i * 128]; up += rd[i * 128 + 64]; }
        const f32x2 gv = unp(pack2(gt)), uv = unp(pack2(up));
        f32x2 sg;
#pragma unroll
        for (int e = 0; e < 2; ++e) sg[e] = gv[e] * __builtin_amdgcn_rcpf(1.0f + __expf(-gv[e]));
        const unsigned o = pack2(unp(pack2(sg)) * uv);
        __builtin_amdgcn_raw_buffer_store_b32(o, rws, gu_u * 256 + (l >> 5) * 128 + ((l >> 1) & 7) * 16 + ((l >> 4) & 1) * 8 + (l & 1) * 4,
                                              SM_HF + r * 32768, 16);
      }
      BAR();
    }
    STAMP(1 + r, 12);
    if (r == 0) qkv_gemm<1>(sm, rws, w0, tag_lo, tag_hi, Wg, Wu, Wd);
    STAMP(1 + r, 13);
    v4u srec;
    {
      IDS
      v4u LAS* scr = (v4u LAS*)(sm + L_SH + 32000);
      if (w == 7) {
        v4u f;
        _Pragma("unroll 1") for (unsigned spins = 0; spins < SPIN; ++spins) {
          f = LD128(xcd * 128 + (l & 7) * 16, SM_IDX + r * 1024);
          if (__all(valid4(f))) break;
          SLEEP(4);
        }
        if (l < 8) scr[l] = f;
      }
      BAR();
      srec = scr[l & 7];
    }
    const int stok = (0xfffe - (int)(srec[0] & 0xffffu)) & 2047;
    if (role == 2) {
      IDS
      v4u ffw[7];
#pragma unroll
      for (int i = 0; i < 7; ++i) ffw[i] = LD128(lane16, OFF_DNF + (dn_u * 56 + 7 * w + i) * 1024);
      v2u xa, aa;
      const unsigned ro = (unsigned)(stok * 1024 + dn_u * 32 + 8 * g4);
      _Pragma("unroll 1") for (unsigned spins = 0; spins < SPIN; ++spins) {
        xa = r == 0 ? *(const v2u*)((const char*)X + ro) : __builtin_amdgcn_raw_buffer_load_b64(rws, ro, OFF_X2, 16);
        aa = __builtin_amdgcn_raw_buffer_load_b64(rws, ro, OFF_AA + r * (2u << 20), 16);
        if (__all(max(aa[0], aa[1]) != SENT)) break;
        SLEEP(2);
      }
      v4u hf[7];
      _Pragma("unroll 1") for (unsigned spins = 0; spins < SPIN; ++spins) {
        unsigned mx = 0;
#pragma unroll
        for (int s = 0; s < 7; ++s) {
          hf[s] = LD128((7 * w + s) * 512 + g4 * 128 + (l & 7) * 16, SM_HF + r * 32768);
          mx = max(mx, umax4(hf[s]));
        }
        if (__all(mx != SENT)) break;
        SLEEP(3);
      }
      STAMP(1 + r, 14);
      f32x4 acc = {0, 0, 0, 0};
#pragma unroll
      for (int s = 0; s < 7; ++s) acc = mfma(ffw[s], hf[s], acc);
      f32x4 LAS* red = (f32x4 LAS*)(sm + L_OS + 16384);
      red[w * 64 + l] = acc;
      BAR();
      if (w == 0 && tok < 8) {
        f32x4 y = red[l];
#pragma unroll
        for (int i = 1; i < 8; ++i) y += red[i * 64 + l];
        unsigned u = srec[0] >> 16;
        u = (u & 0x8000u) ? (u ^ 0x8000u) : (u ^ 0xffffu);
        const float rr = __uint_as_float(u << 16);
        const float gt = rbf(__builtin_amdgcn_rcpf(1.0f + __expf(-rr)));
        const float mb = __uint_as_float(srec[1]), ri = __uint_as_float(srec[2]);
        v2u o;
#pragma unroll
        for (int e = 0; e < 2; ++e) {
          const f32x2 x1 = unp(pack2(unp(xa[e]) + unp(aa[e])));
          const f32x2 y1 = unp(norm2(x1, mb, ri));
          const f32x2 up = unp(pack2(unp(pack2(y[2 * e], y[2 * e + 1])) * gt));
          o[e] = pack2(x1 + unp(pack2(y1 + up)));
        }
        if (r == 0) __builtin_amdgcn_raw_buffer_store_b64(o, rws, tok * 1024 + dn_u * 32 + 8 * g4, SM_X2S, 16);
        else *(v2u*)((char*)Out + ro) = o;
      }
      BAR();
    }
    {
      IDS
      const unsigned long long own = __ballot(stok == myrow);
      if (r == 0) {
        const int lo = 128 * t - 16;
        const bool inr = stok >= lo && stok < lo + 144;
        const unsigned inmask = (unsigned)__ballot(inr) & 0xffu;
        if (inmask) {
          if ((inmask >> w) & 1) {
            v4u xv;
            _Pragma("unroll 1") for (unsigned spins = 0; spins < SPIN; ++spins) {
              xv = LD128(lane16, SM_X2S + w * 1024);
              if (__all(valid4(xv))) break;
              SLEEP(3);
            }
            f32x2 x[4] = {unp(xv[0]), unp(xv[1]), unp(xv[2]), unp(xv[3])};
            float mb, ri;
            *(v4u LAS*)(sm + L_OS + l * 128 + w * 16) = ln_row(x, mb, ri);
          }
          BAR();
          if (w < 6) {
            f32x4 acc = {0, 0, 0, 0};
#pragma unroll
            for (int s = 0; s < 16; ++s)
              acc = mfma(*(const v4u LAS*)(sm + L_W + (6 * s + w) * 1024 + lane16),
                         *(const v4u LAS*)(sm + L_OS + (4 * s + g4) * 128 + (l & 7) * 16), acc);
            if (inr && tok < 8)
              *(v2u LAS*)(sm + L_SH + (2 * w + (g4 >> 1)) * PLANE + 8 * (g4 & 1) + (stok - lo) * 16) = packacc(acc);
          }
          if (own) {
            const int slot = __builtin_ctzll(own);
            _Pragma("unroll 1") for (unsigned spins = 0; spins < SPIN; ++spins) {
              xres = LD128(lane16, SM_X2S + slot * 1024);
              if (__all(valid4(xres))) break;
              SLEEP(2);
            }
            ST128(xres, lane16, OFF_X2 + myrow * 1024);
          }
          BAR();
        }
        STAMP(2, 2);
        attention(sm, rws, w0, 1);
      } else if (!own) {
        *(v4u*)((char*)Out + myrow * 1024 + lane16) = xo;
      }
    }
    STAMP(1 + r, 15);
  }
}

extern "C" void kernel_launch(void* const* d_in, const int* in_sizes, int n_in, void* d_out, int out_size,
                              void* d_ws, size_t ws_size, hipStream_t stream) {
  (void)hipFuncSetAttribute((const void*)k_main, hipFuncAttributeMaxDynamicSharedMemorySize, L_TOTAL);
  k_main<<<256, 512, L_TOTAL, stream>>>((const u16*)d_in[0], (const u16*)d_in[1], (const u16*)d_in[2], (const u16*)d_in[4],
                                        (const u16*)d_in[5], (const u16*)d_in[6], (const u16*)d_in[7], (u16*)d_out, (char*)d_ws);
}
```

```cpp
#include <hip/hip_runtime.h>
#include <hip/hip_bf16.h>
#include <stdint.h>

#define LAS __attribute__((address_space(3)))
typedef unsigned short u16;
typedef short v4s __attribute__((ext_vector_type(4)));
typedef short v8s __attribute__((ext_vector_type(8)));
typedef __bf16 v2bf __attribute__((ext_vector_type(2)));
typedef float f32x4 __attribute__((ext_vector_type(4)));
typedef float f32x2 __attribute__((ext_vector_type(2)));
typedef float f32x16 __attribute__((ext_vector_type(16)));
typedef unsigned v4u __attribute__((ext_vector_type(4)));
typedef unsigned v2u __attribute__((ext_vector_type(2)));


constexpr int S = 2048, D = 512, F = 1792;
constexpr unsigned SENT = 0xFFFFFFFFu;
constexpr unsigned SPIN = 1u << 18;
constexpr unsigned OFF_Z1 = 0, OFF_Z2 = 2u << 20, OFF_OF = 4u << 20, OFF_AA = 8u << 20, OFF_X2 = 12u << 20,
                   OFF_SM = 14u << 20, OFF_WF = 16u << 20, OFF_WOF = 18u << 20, OFF_GUF = 19u << 20,
                   OFF_DNF = 23u << 20, WS_BYTES = 25u << 20;
constexpr unsigned SM_REC = OFF_SM, SM_HF = OFF_SM + 65536, SM_X2S = OFF_SM + 131072, SM_IDX = OFF_SM + 139264;
constexpr unsigned FL_A = OFF_SM + 262144;
constexpr unsigned FL_B = FL_A + 65536;
constexpr unsigned FL_C = FL_B + 4096;
constexpr unsigned FL_E = FL_C + 4096;
constexpr int L_W = 0, L_SH = 98304, L_OS = 131072, L_TOTAL = 163840;
constexpr int PLANE = 2304;
constexpr unsigned CODE_BYTES = 28672;

__device__ __forceinline__ float bf_lo(unsigned u) { return __uint_as_float(u << 16); }
__device__ __forceinline__ float bf_hi(unsigned u) { return __uint_as_float(u & 0xffff0000u); }
__device__ __forceinline__ unsigned pack2(f32x2 f) {
  v2bf c = __builtin_convertvector(f, v2bf);
  return __builtin_bit_cast(unsigned, c);
}
__device__ __forceinline__ unsigned pack2(float a, float b) { return pack2((f32x2){a, b}); }
__device__ __forceinline__ f32x2 unp(unsigned u) { return (f32x2){bf_lo(u), bf_hi(u)}; }
__device__ __forceinline__ float rbf(float a) {
  __bf16 c = (__bf16)a;
  return __uint_as_float(((unsigned)__builtin_bit_cast(u16, c)) << 16);
}
__device__ __forceinline__ float dot2(unsigned a, unsigned b, float c) {
  return __builtin_amdgcn_fdot2_f32_bf16(__builtin_bit_cast(v2bf, a), __builtin_bit_cast(v2bf, b), c, false);
}
__device__ __forceinline__ v4u tr2(const char LAS* p, int second) {
  const v4s a = __builtin_amdgcn_ds_read_tr16_b64_v4i16((LAS v4s*)p);
  const v4s b = __builtin_amdgcn_ds_read_tr16_b64_v4i16((LAS v4s*)(p + second));
  const v8s r = {a[0], a[1], a[2], a[3], b[0], b[1], b[2], b[3]};
  return __builtin_bit_cast(v4u, r);
}
__device__ __forceinline__ f32x4 mfma(v4u a, v4u b, f32x4 c) {
  return __builtin_amdgcn_mfma_f32_16x16x32_bf16(__builtin_bit_cast(v8s, a), __builtin_bit_cast(v8s, b), c, 0, 0, 0);
}
__device__ __forceinline__ v2u packacc(f32x4 a) { return (v2u){pack2(a[0], a[1]), pack2(a[2], a[3])}; }
typedef __amdgpu_buffer_rsrc_t srd_t;
__device__ __forceinline__ srd_t make_srd(const void* p, unsigned bytes) {
  return __builtin_amdgcn_make_buffer_rsrc((void*)p, 0, bytes, 0x00020000);
}
__device__ __forceinline__ void dma16(srd_t srd, unsigned voff, unsigned soff, unsigned lds) {
  unsigned keep;
  asm volatile("s_mov_b32 %0, m0\n\ts_mov_b32 m0, %3\n\ts_nop 0\n\tbuffer_load_dwordx4 %1, %2, %4 offen lds\n\ts_mov_b32 m0, %0"
               : "=&s"(keep) : "v"(voff), "s"(srd), "s"(lds), "s"(soff));
}
__device__ __forceinline__ void dma16_sc1(srd_t srd, unsigned voff, unsigned soff, unsigned lds) {
  unsigned keep;
  asm volatile("s_mov_b32 %0, m0\n\ts_mov_b32 m0, %3\n\ts_nop 0\n\tbuffer_load_dwordx4 %1, %2, %4 offen sc1 lds\n\ts_mov_b32 m0, %0"
               : "=&s"(keep) : "v"(voff), "s"(srd), "s"(lds), "s"(soff));
}
__device__ __forceinline__ void touch_sc1(srd_t srd, unsigned voff, unsigned soff, unsigned lds) {
  unsigned keep;
  asm volatile("s_mov_b32 %0, m0\n\ts_mov_b32 m0, %3\n\ts_nop 0\n\tbuffer_load_dword %1, %2, %4 offen sc1 lds\n\ts_mov_b32 m0, %0"
               : "=&s"(keep) : "v"(voff), "s"(srd), "s"(lds), "s"(soff));
}
__device__ __forceinline__ void aload16(v4u& d, srd_t srd, unsigned voff, unsigned soff) {
  asm volatile("buffer_load_dwordx4 %0, %1, %2, %3 offen" : "=v"(d) : "v"(voff), "s"(srd), "s"(soff));
}
__device__ __forceinline__ void aload16_sc1(v4u& d, srd_t srd, unsigned voff, unsigned soff) {
  asm volatile("buffer_load_dwordx4 %0, %1, %2, %3 offen sc1" : "=v"(d) : "v"(voff), "s"(srd), "s"(soff));
}
__device__ __forceinline__ void astore16_sc1(v4u d, srd_t srd, unsigned voff, unsigned soff) {
  asm volatile("buffer_store_dwordx4 %0, %1, %2, %3 offen sc1\n\ts_nop 1" :: "v"(d), "v"(voff), "s"(srd), "s"(soff));
}
#define SLEEP(n) do { __builtin_amdgcn_s_sleep(n); asm volatile("" ::: "memory"); } while (0)
#define VMWAIT(n) asm volatile("s_waitcnt vmcnt(" #n ")" ::: "memory")
#define BAR() do { asm volatile("s_waitcnt lgkmcnt(0)" ::: "memory"); __builtin_amdgcn_s_barrier(); asm volatile("" ::: "memory"); } while (0)
#define LD128(voff, soff) __builtin_amdgcn_raw_buffer_load_b128(rws, voff, soff, 16)
__device__ __forceinline__ void st128(v4u d, srd_t srd, unsigned voff, unsigned soff) {
  asm volatile("buffer_store_dwordx4 %0, %1, %2, %3 offen sc1\n\ts_nop 1" :: "v"(d), "v"(voff), "s"(srd), "s"(soff) : "memory");
}
#define ST128(v, voff, soff) st128(v, rws, voff, soff)
__device__ __forceinline__ unsigned umax4(v4u v) { return max(max(v[0], v[1]), max(v[2], v[3])); }
__device__ __forceinline__ bool valid4(v4u v) { return umax4(v) != SENT; }

template <int CTRL, int RMASK>
__device__ __forceinline__ float dpp_add(float v) {
  int t = __builtin_amdgcn_update_dpp(0, __float_as_int(v), CTRL, RMASK, 0xf, true);
  return v + __int_as_float(t);
}
__device__ __forceinline__ float wave_sum(float v) {
  v = dpp_add<0x111, 0xf>(v); v = dpp_add<0x112, 0xf>(v); v = dpp_add<0x114, 0xf>(v); v = dpp_add<0x118, 0xf>(v);
  v = dpp_add<0x142, 0xa>(v); v = dpp_add<0x143, 0xc>(v);
  return __int_as_float(__builtin_amdgcn_readlane(__float_as_int(v), 63));
}
template <int CTRL, int RMASK>
__device__ __forceinline__ unsigned dpp_max(unsigned v) {
  unsigned t = (unsigned)__builtin_amdgcn_update_dpp(0, (int)v, CTRL, RMASK, 0xf, true);
  return v > t ? v : t;
}
__device__ __forceinline__ unsigned wave_max(unsigned v) {
  v = dpp_max<0x111, 0xf>(v); v = dpp_max<0x112, 0xf>(v); v = dpp_max<0x114, 0xf>(v); v = dpp_max<0x118, 0xf>(v);
  v = dpp_max<0x142, 0xa>(v); v = dpp_max<0x143, 0xc>(v);
  return (unsigned)__builtin_amdgcn_readlane((int)v, 63);
}
__device__ __forceinline__ unsigned norm2(f32x2 x, float mb, float ri) { return pack2(unp(pack2(x - mb)) * ri); }
__device__ __forceinline__ v4u ln_row(const f32x2 (&x)[4], float& mb, float& ri) {
  const float c = __int_as_float(__builtin_amdgcn_readfirstlane(__float_as_int(x[0][0])));
  const f32x2 d0 = x[0] - c, d1 = x[1] - c, d2 = x[2] - c, d3 = x[3] - c;
  const f32x2 s2 = (d0 + d1) + (d2 + d3);
  const f32x2 q2 = (d0 * d0 + d1 * d1) + (d2 * d2 + d3 * d3);
  float s = s2[0] + s2[1], q = q2[0] + q2[1];
  s = dpp_add<0x111, 0xf>(s); q = dpp_add<0x111, 0xf>(q);
  s = dpp_add<0x112, 0xf>(s); q = dpp_add<0x112, 0xf>(q);
  s = dpp_add<0x114, 0xf>(s); q = dpp_add<0x114, 0xf>(q);
  s = dpp_add<0x118, 0xf>(s); q = dpp_add<0x118, 0xf>(q);
  s = dpp_add<0x142, 0xa>(s); q = dpp_add<0x142, 0xa>(q);
  s = dpp_add<0x143, 0xc>(s); q = dpp_add<0x143, 0xc>(q);
  const float dm = __int_as_float(__builtin_amdgcn_readlane(__float_as_int(s), 63)) * (1.0f / D);
  const float var = fmaxf(__int_as_float(__builtin_amdgcn_readlane(__float_as_int(q), 63)) * (1.0f / D) - dm * dm, 0.f);
  mb = rbf(c + dm);
  const float sg = rbf(__builtin_amdgcn_sqrtf(rbf(rbf(var) + 1e-5f)));
  ri = __builtin_amdgcn_rcpf(sg);
  return (v4u){norm2(x[0], mb, ri), norm2(x[1], mb, ri), norm2(x[2], mb, ri), norm2(x[3], mb, ri)};
}

#define IDS                                                                                              \
  int tid = threadIdx.x, w = w0, b = blockIdx.x;                                                         \
  asm volatile("" : "+v"(tid)); asm volatile("" : "+s"(w)); asm volatile("" : "+s"(b));                  \
  const int l = tid & 63, xcd = b & 7, jj = b >> 3;                                                      \
  const int t = 4 * (xcd >> 1) + (jj >> 3), j = 8 * (xcd & 1) + (jj & 7);                                \
  const int g4 = l >> 4, tok = l & 15;                                                                   \
  const int rq = j >> 2, cq = j & 3, myrow = 128 * t + 8 * j + w;                                        \
  const int gu_u = 14 * xcd + jj, dn_u = 4 * xcd + (jj - 14);                                            \
  const unsigned lane16 = l * 16;                                                                        \
  const int rth = t > 0 ? 8 * t - 1 : 0;                                                                 \
  (void)rq; (void)cq; (void)myrow; (void)gu_u; (void)dn_u; (void)lane16; (void)rth; (void)tok; (void)g4;

__device__ __forceinline__ void attention(char LAS* sm, srd_t rws, int w0, int r) {
  IDS
  const char LAS* rp = sm + L_SH + g4 * PLANE + (16 + 16 * w + tok) * 16;
  const v4u q0 = *(const v4u LAS*)rp;
  float d[8];
#pragma unroll
  for (int k = 0; k < 8; ++k) {
    const v4u k0 = *(const v4u LAS*)(rp + 4 * PLANE - 32 * k);
    d[k] = dot2(q0[3], k0[3], dot2(q0[2], k0[2], dot2(q0[1], k0[1], dot2(q0[0], k0[0], 0.f))));
  }
  float lg[4];
#pragma unroll
  for (int k = 0; k < 4; ++k) {
    const auto sw = __builtin_amdgcn_permlane16_swap(__float_as_uint(d[k]), __float_as_uint(d[k + 4]), false, false);
    lg[k] = rbf(__uint_as_float(sw[0]) + __uint_as_float(sw[1]));
  }
  if (t == 0) {
#pragma unroll
    for (int k = 0; k < 4; ++k) lg[k] = (16 * w + tok - 2 * k - 8 * (g4 & 1) >= 0) ? lg[k] : -4e9f;
  }
  float m = fmaxf(fmaxf(lg[0], lg[1]), fmaxf(lg[2], lg[3]));
  {
    const auto sw = __builtin_amdgcn_permlane16_swap(__float_as_uint(m), __float_as_uint(m), false, false);
    m = fmaxf(__uint_as_float(sw[0]), __uint_as_float(sw[1]));
  }
  constexpr float C = 0.25f * 1.44269504088896341f;
#pragma unroll
  for (int k = 0; k < 4; ++k) lg[k] = __builtin_amdgcn_exp2f((lg[k] - m) * C);
  float sum = (lg[0] + lg[1]) + (lg[2] + lg[3]);
  {
    const auto sw = __builtin_amdgcn_permlane16_swap(__float_as_uint(sum), __float_as_uint(sum), false, false);
    sum = __uint_as_float(sw[0]) + __uint_as_float(sw[1]);
  }
  const float inv = __builtin_amdgcn_rcpf(sum);
  const unsigned pa = pack2(lg[0] * inv, lg[1] * inv), pb = pack2(lg[2] * inv, lg[3] * inv);
  const auto sa = __builtin_amdgcn_permlane16_swap(pa, pa, false, false);
  const auto sb = __builtin_amdgcn_permlane16_swap(pb, pb, false, false);
  const unsigned pp[4] = {sa[0], sb[0], sa[1], sb[1]};
  float o[8];
#pragma unroll
  for (int e = 0; e < 8; ++e) o[e] = 0.f;
#pragma unroll
  for (int k = 0; k < 4; ++k) {
    const v4u v0 = *(const v4u LAS*)(rp + 8 * PLANE - 64 * k), v1 = *(const v4u LAS*)(rp + 8 * PLANE - 64 * k - 32);
#pragma unroll
    for (int e = 0; e < 4; ++e) {
      o[2 * e] = dot2(pp[k], __builtin_amdgcn_perm(v1[e], v0[e], 0x05040100u), o[2 * e]);
      o[2 * e + 1] = dot2(pp[k], __builtin_amdgcn_perm(v1[e], v0[e], 0x07060302u), o[2 * e + 1]);
    }
  }
  const v4u ov = {pack2(o[0], o[1]), pack2(o[2], o[3]), pack2(o[4], o[5]), pack2(o[6], o[7])};
  ST128(ov, lane16, OFF_OF + r * (2u << 20) + ((8 * t + w) * 16 + j) * 1024);
}


struct Unit { srd_t srd; unsigned stride, dst, dstep, dodd; };
__device__ __forceinline__ Unit ffn_unit(int u, const u16* Wg, const u16* Wu, const u16* Wd) {
  Unit r;
  const char* src = (const char*)Wd;
  r.stride = 1024; r.dst = WS_BYTES; r.dstep = 0; r.dodd = 0;
  if (u < 896) {
    const int up = u >= 448, v = u - 448 * up, s = v / 28, nb = v % 28;
    src = (const char*)(up ? Wu : Wg) + (size_t)(32 * s) * (F * 2) + nb * 128; r.stride = F * 2;
    r.dst = OFF_GUF + ((4 * nb * 16 + s) * 2 + up) * 1024; r.dstep = 2 * 32 * 1024; r.dodd = 32 * 1024;
  } else if (u < 1344) {
    const int s = (u - 896) >> 3, nb = u & 7;
    src = (const char*)Wd + (size_t)(32 * s) * 1024 + nb * 128;
    r.dst = OFF_DNF + (4 * nb * 56 + s) * 1024; r.dstep = 2 * 56 * 1024; r.dodd = 56 * 1024;
  }
  r.srd = make_srd(src, u < 1344 ? 32 * F * 2 : 0);
  return r;
}
__device__ __forceinline__ void unit_store(char LAS* scr, srd_t rws, int l, const v4u (&wv)[4], unsigned dst, unsigned dstep, unsigned dodd) {
#pragma unroll
  for (int i = 0; i < 4; ++i) *(v4u LAS*)(scr + i * 1024 + l * 16) = wv[i];
  asm volatile("s_waitcnt lgkmcnt(0)" ::: "memory");
  const char LAS* p = scr + (8 * (l >> 4) + ((l & 15) >> 2)) * 128 + 8 * (l & 3);
  v4s a[4], c[4];
#pragma unroll
  for (int i = 0; i < 4; ++i) {
    a[i] = __builtin_amdgcn_ds_read_tr16_b64_v4i16((LAS v4s*)(p + 32 * i));
    c[i] = __builtin_amdgcn_ds_read_tr16_b64_v4i16((LAS v4s*)(p + 32 * i + 512));
  }
#pragma unroll
  for (int i = 0; i < 4; ++i) {
    const v8s f = {a[i][0], a[i][1], a[i][2], a[i][3], c[i][0], c[i][1], c[i][2], c[i][3]};
    astore16_sc1(__builtin_bit_cast(v4u, f), rws, l * 16, dst + (i >> 1) * dstep + (i & 1) * dodd);
  }
  asm volatile("" ::: "memory");
}
__device__ __forceinline__ void wait_all_flags(srd_t rws, int l, unsigned off, unsigned lo, unsigned hi) {
  _Pragma("unroll 1") for (unsigned spins = 0; spins < SPIN; ++spins) {
    const v4u a = LD128(l * 32, off), c = LD128(l * 32 + 16, off);
    if (__all((((a[0] ^ lo) | (a[1] ^ hi)) | ((a[2] ^ lo) | (a[3] ^ hi)) | ((c[0] ^ lo) | (c[1] ^ hi)) | ((c[2] ^ lo) | (c[3] ^ hi))) == 0u)) break;
    SLEEP(3);
  }
}
constexpr int L_CTL = L_SH + 32704;
#define WFRAG(s, ct) ((6 * (s) + (ct)) * 1024)

#define WAIT_TIE4(n, a, i) do { VMWAIT(n); __builtin_amdgcn_sched_barrier(0); asm volatile("" : "+v"(a[i]), "+v"(a[i + 1]), "+v"(a[i + 2]), "+v"(a[i + 3])); } while (0)
template <int R>
__device__ __forceinline__ void qkv_gemm(char LAS* sm, srd_t rws, int w0, unsigned tag_lo, unsigned tag_hi,
                                         const u16* Wg, const u16* Wu, const u16* Wd) {
  IDS
  if (R == 1 && tid == 64) *(unsigned LAS*)(sm + L_CTL) = 0u;
  if (w == 0) {
    int fi = l < 16 ? 16 * t + l : (t > 0 ? 16 * t - 2 + (l & 1) : 0);
    if (R == 0 && l >= 32) fi = 16 * ((l >> 1) & 15) + (j & 14) + (l & 1);
    _Pragma("unroll 1") for (unsigned spins = 0; spins < SPIN; ++spins) {
      const v2u f = __builtin_amdgcn_raw_buffer_load_b64(rws, fi * 128, FL_A + R * 32768, 16);
      if (__all(((f[0] ^ (tag_lo + R)) | (f[1] ^ tag_hi)) == 0u)) break;
      SLEEP(3);
    }
  }
  BAR();
  const unsigned zo = R ? OFF_Z2 : OFF_Z1;
  if (w < 4) {
    const unsigned lo = (unsigned)(((l >> 4) & 1) * 16384 + (l >> 5) * 256 + tok * 16);
    const unsigned zb = zo + (8 * t + 2 * w) * 16384;
    v4u z[32];
#pragma unroll
    for (int u = 0; u < 8; ++u) aload16_sc1(z[u], rws, lo, zb + u * 512);
    const char LAS* wp = sm + L_W + ((l >> 4) & 1) * 1024 + (l >> 5) * 256 + tok * 16;
    f32x16 acc[3];
#pragma unroll
    for (int c = 0; c < 3; ++c)
#pragma unroll
      for (int e = 0; e < 16; ++e) acc[c][e] = 0.f;
    v4u f[3][3];
    if (R == 0) BAR();
#pragma unroll
    for (int u = 8; u < 12; ++u) aload16_sc1(z[u], rws, lo, zb + u * 512);
#pragma unroll
    for (int u = 0; u < 2; ++u)
#pragma unroll
      for (int c = 0; c < 3; ++c) f[u][c] = *(const v4u LAS*)(wp + (6 * (u >> 1) + 2 * c) * 1024 + (u & 1) * 512);
#pragma unroll
    for (int u = 0; u < 32; ++u) {
      if (!(u & 3)) { if (u < 24) WAIT_TIE4(8, z, u); else if (u == 24) WAIT_TIE4(4, z, 24); else WAIT_TIE4(0, z, 28); }
      if (R == 0 && (u & 7) == 6 && u < 30) { asm volatile("" ::: "memory"); __builtin_amdgcn_s_barrier(); asm volatile("" ::: "memory"); }
      if (u < 30) {
#pragma unroll
        for (int c = 0; c < 3; ++c)
          f[(u + 2) % 3][c] = *(const v4u LAS*)(wp + (6 * ((u + 2) >> 1) + 2 * c) * 1024 + (u & 1) * 512);
      }
      __builtin_amdgcn_sched_barrier(0);
#pragma unroll
      for (int c = 0; c < 3; ++c)
        acc[c] = __builtin_amdgcn_mfma_f32_32x32x16_bf16(__builtin_bit_cast(v8s, f[u % 3][c]), __builtin_bit_cast(v8s, z[u]), acc[c], 0, 0, 0);
      if (u < 20) aload16_sc1(z[u + 12], rws, lo, zb + (u + 12) * 512);
      __builtin_amdgcn_sched_barrier(0);
    }
    BAR();
    char LAS* qp = sm + L_SH + (16 + 32 * w + (l & 31)) * 16 + 8 * (l >> 5);
#pragma unroll
    for (int c = 0; c < 3; ++c)
#pragma unroll
      for (int g = 0; g < 4; ++g)
        *(v2u LAS*)(qp + (4 * c + g) * PLANE) =
            (v2u){pack2(acc[c][4 * g], acc[c][4 * g + 1]), pack2(acc[c][4 * g + 2], acc[c][4 * g + 3])};
  } else {
    const int h = w - 4;
    v4u zh[4];
#pragma unroll
    for (int i = 0; i < 4; ++i) aload16_sc1(zh[i], rws, lane16, zo + (rth * 16 + 4 * h + i) * 1024);
    f32x4 acch = {0, 0, 0, 0};
    const char LAS* wh = sm + L_W + (2 + h) * 1024 + lane16;
    const char LAS* hp = sm + L_SH + lane16;
    if (R == 0) {
      const unsigned wb = OFF_WF + j * 98304 + 6 * h * 1024;
      v4u fv[2][4];
      const Unit u0 = ffn_unit(h * 256 + b, Wg, Wu, Wd), u1 = ffn_unit(1024 + h * 256 + b, Wg, Wu, Wd);
#pragma unroll
      for (int q = 0; q < 4; ++q) {
        if (q == 0) {
#pragma unroll
          for (int p = 0; p < 6; ++p) dma16_sc1(rws, lane16, wb + p * 1024, L_W + (6 * h + p) * 1024);
        }
        if (q < 3) {
#pragma unroll
          for (int p = 0; p < 6; ++p) dma16_sc1(rws, lane16, wb + (24 * (q + 1) + p) * 1024, L_W + (24 * (q + 1) + 6 * h + p) * 1024);
        }
        if (q == 2) {
#pragma unroll
          for (int i = 0; i < 4; ++i) aload16(fv[0][i], u0.srd, (unsigned)(l >> 3) * u0.stride + (l & 7) * 16, 8 * i * u0.stride);
#pragma unroll
          for (int i = 0; i < 4; ++i) aload16(fv[1][i], u1.srd, (unsigned)(l >> 3) * u1.stride + (l & 7) * 16, 8 * i * u1.stride);
        }
        if (q == 0) {
          WAIT_TIE4(6, zh, 0);
#pragma unroll
          for (int i = 0; i < 4; ++i) *(v4u LAS*)(sm + L_SH + (4 * h + i) * 1024 + lane16) = zh[i];
        } else if (q == 1) VMWAIT(6);
        else if (q == 2) VMWAIT(14);
        else VMWAIT(8);
        BAR();
        if (q == 0) {
          if (tid == 448) __builtin_amdgcn_raw_buffer_store_b64((v2u){tag_lo + 2, tag_hi}, rws, b * 8, FL_B, 16);
        }
      }
#pragma unroll
      for (int s = 0; s < 16; ++s) acch = mfma(*(const v4u LAS*)(wh + s * 6144), *(const v4u LAS*)(hp + s * 1024), acch);
      WAIT_TIE4(4, fv[0], 0);
      unit_store(sm + L_SH + 16384 + h * 4096, rws, l, fv[0], u0.dst, u0.dstep, u0.dodd);
      WAIT_TIE4(4, fv[1], 0);
      unit_store(sm + L_SH + 16384 + h * 4096, rws, l, fv[1], u1.dst, u1.dstep, u1.dodd);
    } else {
      WAIT_TIE4(0, zh, 0);
#pragma unroll
      for (int i = 0; i < 4; ++i) *(v4u LAS*)(sm + L_SH + (4 * h + i) * 1024 + lane16) = zh[i];
      asm volatile("s_waitcnt lgkmcnt(0)" ::: "memory");
      if (l == 0) __hip_atomic_fetch_add((unsigned LAS*)(sm + L_CTL), 1u, __ATOMIC_RELAXED, __HIP_MEMORY_SCOPE_WORKGROUP);
      volatile unsigned LAS* ctl = (volatile unsigned LAS*)(sm + L_CTL);
      _Pragma("unroll 1") for (unsigned spins = 0; spins < (SPIN << 4) && *ctl < 4u; ++spins) SLEEP(1);
      asm volatile("" ::: "memory");
#pragma unroll
      for (int s = 0; s < 16; ++s) acch = mfma(*(const v4u LAS*)(wh + s * 6144), *(const v4u LAS*)(hp + s * 1024), acch);
    }
    BAR();
    *(v2u LAS*)(sm + L_SH + (2 * (2 + h) + (g4 >> 1)) * PLANE + 8 * (g4 & 1) + tok * 16) = packacc(acch);
  }
}

__global__ __launch_bounds__(512, 2) void k_main(const u16* __restrict__ X, const u16* __restrict__ Wqkv,
                                                 const u16* __restrict__ Wo, const u16* __restrict__ Wr,
                                                 const u16* __restrict__ Wg, const u16* __restrict__ Wu,
                                                 const u16* __restrict__ Wd, u16* __restrict__ Out,
                                                 char* __restrict__ ws) {
  extern __shared__ __attribute__((aligned(16))) char smem[];
  char LAS* sm = (char LAS*)smem;
  asm volatile("" :: "s"(X), "s"(Wqkv), "s"(Wo), "s"(Wr), "s"(Wg), "s"(Wu), "s"(Wd), "s"(Out), "s"(ws));
  const int w0 = __builtin_amdgcn_readfirstlane(threadIdx.x >> 6);
  const srd_t rws = make_srd(ws, WS_BYTES);
  const int role = (blockIdx.x >> 3) < 14 ? 1 : (blockIdx.x >> 3) < 18 ? 2 : 0;
  if (w0 == 7 && (blockIdx.x >> 3) < 4) {
    unsigned long long pc;
    asm volatile("s_getpc_b64 %0" : "=s"(pc));
    touch_sc1(make_srd((const void*)pc, CODE_BYTES), (threadIdx.x & 63) * 128, (blockIdx.x >> 3) * 8192, L_SH + 32256);
  }
  v4u xres, wrv;
  v4u wof[16];
  unsigned tag_lo, tag_hi, ep_lo, ep_hi;
  {
    IDS
    v2u ev;
    v4u wv[4];
    asm volatile("buffer_load_dwordx2 %0, off, %1, %2 sc1" : "=v"(ev) : "s"(rws), "s"(FL_E));
    aload16(wrv, make_srd(Wr, D * 2), lane16, 0);
    aload16(xres, make_srd(X, S * D * 2), lane16, myrow * 1024);
    const bool has_unit = w == 0 || (w == 1 && !(j & 1));
    const int up = w == 0 ? (j & 1) : 2;
    {
      const srd_t su = make_srd((const char*)Wqkv + (size_t)(32 * t) * 3072 + up * 1024 + (j >> 1) * 128, has_unit ? 31 * 3072 + 128 : 0);
#pragma unroll
      for (int i = 0; i < 4; ++i) aload16(wv[i], su, (unsigned)(l >> 3) * 3072 + (l & 7) * 16, 8 * i * 3072);
    }
    const srd_t srd_wo = make_srd(Wo, D * D * 2);
    const unsigned wovoff = (unsigned)(g4 * 1024 + ((tok ^ ((g4 | (((w >> 1) & 1) << 2)) << 1)) * 16));
#pragma unroll
    for (int q = 0; q < 2; ++q)
#pragma unroll
      for (int m = 0; m < 4; ++m)
        dma16(srd_wo, wovoff, (unsigned)((128 * q + 4 * (w + 8 * m)) * 1024 + cq * 256), L_W + q * 32768 + (w + 8 * m) * 1024);
    if (w == 0) {
#pragma unroll
      for (int m = 0; m < 4; ++m) dma16(srd_wo, wovoff, (unsigned)((256 + 32 * m) * 1024 + cq * 256), L_OS + 8 * m * 1024);
      VMWAIT(16);
    } else {
      VMWAIT(12);
    }
    __builtin_amdgcn_sched_barrier(0);
    asm volatile("" : "+v"(ev), "+v"(xres), "+v"(wrv));
    ep_lo = __builtin_amdgcn_readfirstlane(ev[0]);
    ep_hi = __builtin_amdgcn_readfirstlane(ev[1]);
    {
      const unsigned long long e = ((unsigned long long)ep_hi << 32) | ep_lo;
      const unsigned long long tg = (e + 1) * 0x9E3779B97F4A7C15ull;
      tag_lo = (unsigned)tg & ~3u; tag_hi = (unsigned)(tg >> 32);
    }
    {
      f32x2 x[4] = {unp(xres[0]), unp(xres[1]), unp(xres[2]), unp(xres[3])};
      float mb, ri;
      *(v4u LAS*)(sm + L_SH + 8192 + w * 1040 + lane16) = ln_row(x, mb, ri);
    }
    if (w == 0) VMWAIT(12); else VMWAIT(8);
    __builtin_amdgcn_sched_barrier(0);
    asm volatile("" : "+v"(wv[0]), "+v"(wv[1]), "+v"(wv[2]), "+v"(wv[3]));
    BAR();
    if (w < 2) {
      if (has_unit) unit_store(sm + L_SH + w * 4096, rws, l, wv, OFF_WF + (((j & 14) * 16 + t) * 6 + 2 * up) * 1024, 98304, 1024);
    } else if (w >= 4) {
#pragma unroll
      for (int i = 0; i < 2; ++i) {
        const int chunk = 16 * (w - 4) + 8 * i + (l >> 3), r8 = l & 7;
        const v4u v = *(const v4u LAS*)(sm + L_SH + 8192 + r8 * 1040 + chunk * 16);
        ST128(v, (((8 * t + (j >> 1)) * 16 + (chunk >> 2)) * 64 + (chunk & 3) * 16 + 8 * (j & 1) + r8) * 16, OFF_Z1);
      }
    }
    VMWAIT(0);
    BAR();
    if (tid == 0) __builtin_amdgcn_raw_buffer_store_b64((v2u){tag_lo, tag_hi}, rws, (16 * t + j) * 128, FL_A, 16);
    if (w) {
#pragma unroll
      for (int m = 0; m < 4; ++m)
        dma16(srd_wo, wovoff, (unsigned)((256 + 4 * (w + 8 * m)) * 1024 + cq * 256), L_OS + (w + 8 * m) * 1024);
    }
    const char LAS* wp = sm + (8 * g4 + (tok >> 2)) * 256 + ((w ^ ((tok >> 2) | ((g4 & 1) << 2))) * 32) + 8 * (l & 3);
#pragma unroll
    for (int s = 0; s < 8; ++s) wof[s] = tr2(wp + L_W + s * 8192, 1024);
    if (w >= 4) {
      const v4u sv = {SENT, SENT, SENT, SENT};
      const int w2 = 2 * (w - 4);
      const unsigned ao = (unsigned)((128 * t + 32 * rq + 4 * w2 + g4) * 1024 + cq * 256 + tok * 16);
      astore16_sc1(sv, rws, lane16, OFF_OF + ((8 * t + w2) * 16 + j) * 1024);
      astore16_sc1(sv, rws, lane16, OFF_OF + ((8 * t + w2 + 1) * 16 + j) * 1024);
      astore16_sc1(sv, rws, ao, OFF_AA);
      astore16_sc1(sv, rws, ao, OFF_AA + 4096);
      if (w == 4) astore16_sc1(sv, rws, l < 36 ? lane16 : WS_BYTES, OFF_SM + b * 576);
    }
  }
  qkv_gemm<0>(sm, rws, w0, tag_lo, tag_hi, Wg, Wu, Wd);
  {
    IDS
    const char LAS* wp = sm + L_OS + (8 * g4 + (tok >> 2)) * 256 + ((w ^ ((tok >> 2) | ((g4 & 1) << 2))) * 32) + 8 * (l & 3);
#pragma unroll
    for (int s = 8; s < 12; ++s) wof[s] = tr2(wp + (s - 8) * 8192, 1024);
    BAR();
    const srd_t srd_wo = make_srd(Wo, D * D * 2);
    const unsigned wovoff = (unsigned)(g4 * 1024 + ((tok ^ ((g4 | (((w >> 1) & 1) << 2)) << 1)) * 16));
#pragma unroll
    for (int m = 0; m < 4; ++m)
      dma16(srd_wo, wovoff, (unsigned)((384 + 4 * (w + 8 * m)) * 1024 + cq * 256), L_OS + (w + 8 * m) * 1024);
    attention(sm, rws, w0, 0);
    VMWAIT(1);
    BAR();
#pragma unroll
    for (int s = 12; s < 16; ++s) wof[s] = tr2(wp + (s - 12) * 8192, 1024);
    BAR();
  }

#pragma unroll 1
  for (int r = 0; r < 2; ++r) {
    {
      IDS
      v4u of[4];
      const unsigned ofb = OFF_OF + r * (2u << 20) + ((8 * t + 2 * rq) * 16 + 4 * w) * 1024;
      if (r == 0) wait_all_flags(rws, l, FL_B, tag_lo + 2, tag_hi);
      else SLEEP(10);
      _Pragma("unroll 1") for (unsigned spins = 0; spins < SPIN; ++spins) {
#pragma unroll
        for (int i = 0; i < 4; ++i) of[i] = LD128(lane16, ofb + i * 1024);
        if (__all(max(max(umax4(of[0]), umax4(of[1])), max(umax4(of[2]), umax4(of[3]))) != SENT)) break;
        SLEEP(2);
      }
#pragma unroll
      for (int i = 0; i < 4; ++i) *(v4u LAS*)(sm + L_OS + (4 * w + i) * 1024 + lane16) = of[i];
    }
    BAR();
    {
      IDS
      f32x4 a0 = {0, 0, 0, 0}, a1 = {0, 0, 0, 0};
      const char LAS* op = sm + L_OS + lane16;
      v4u f[2][8];
#pragma unroll
      for (int i = 0; i < 4; ++i) { f[0][2 * i] = *(const v4u LAS*)(op + i * 1024); f[0][2 * i + 1] = *(const v4u LAS*)(op + (16 + i) * 1024); }
#pragma unroll
      for (int c = 0; c < 4; ++c) {
        if (c < 3) {
#pragma unroll
          for (int i = 0; i < 4; ++i) {
            f[(c + 1) & 1][2 * i] = *(const v4u LAS*)(op + (4 * c + 4 + i) * 1024);
            f[(c + 1) & 1][2 * i + 1] = *(const v4u LAS*)(op + (20 + 4 * c + i) * 1024);
          }
        }
        __builtin_amdgcn_sched_barrier(0);
#pragma unroll
        for (int i = 0; i < 4; ++i) {
          a0 = mfma(wof[4 * c + i], f[c & 1][2 * i], a0);
          a1 = mfma(wof[4 * c + i], f[c & 1][2 * i + 1], a1);
        }
        __builtin_amdgcn_sched_barrier(0);
      }
      char LAS* tp = sm + L_SH + tok * 272 + 32 * w + 8 * g4;
      *(v2u LAS*)tp = packacc(a0);
      *(v2u LAS*)(tp + 16 * 272) = packacc(a1);
      BAR();
      const v4u v = *(const v4u LAS*)(sm + L_SH + (4 * w + g4) * 272 + tok * 16);
      ST128(v, (128 * t + 32 * rq + 4 * w + g4) * 1024 + cq * 256 + tok * 16, OFF_AA + r * (2u << 20));
    }
    v4u xo;
    {
      IDS
      SLEEP(10);
      v4u av;
      _Pragma("unroll 1") for (unsigned spins = 0; spins < SPIN; ++spins) {
        av = LD128(lane16, OFF_AA + r * (2u << 20) + myrow * 1024);
        if (__all(valid4(av))) break;
        SLEEP(2);
      }
      f32x2 x1[4];
      float mb, ri;
#pragma unroll
      for (int e = 0; e < 4; ++e) x1[e] = unp(pack2(unp(xres[e]) + unp(av[e])));
      const v4u y = ln_row(x1, mb, ri);
      const float d = wave_sum(dot2(y[3], wrv[3], dot2(y[2], wrv[2], dot2(y[1], wrv[1], dot2(y[0], wrv[0], 0.f)))));
      unsigned u = __float_as_uint(rbf(d)) >> 16;
      u = (u & 0x8000u) ? (u ^ 0xffffu) : (u ^ 0x8000u);
      v4u rec = {(u << 16) | (unsigned)(0xfffe - myrow), __float_as_uint(mb), __float_as_uint(ri), 0u};
      rec[1] = rec[1] == SENT ? 0x7fc00000u : rec[1];
      rec[2] = rec[2] == SENT ? 0x7fc00000u : rec[2];
      if (l == 0) *(v4u LAS*)(sm + L_SH + 8960 + w * 16) = rec;
      BAR();
      if (tid < 8) ST128(*(const v4u LAS*)(sm + L_SH + 8960 + tid * 16), (128 * t + 8 * j + tid) * 16, SM_REC + r * 32768);
#pragma unroll
      for (int e = 0; e < 4; ++e) xo[e] = pack2(x1[e] + unp(y[e]));
      if (r == 0) {
        xres = xo;
        ST128(xo, lane16, OFF_X2 + myrow * 1024);
#pragma unroll
        for (int e = 0; e < 4; ++e) x1[e] = unp(xo[e]);
        *(v4u LAS*)(sm + L_SH + 9216 + w * 1040 + lane16) = ln_row(x1, mb, ri);
        BAR();
        const int chunk = 8 * w + (l >> 3), r8 = l & 7;
        const v4u v = *(const v4u LAS*)(sm + L_SH + 9216 + r8 * 1040 + chunk * 16);
        ST128(v, (((8 * t + (j >> 1)) * 16 + (chunk >> 2)) * 64 + (chunk & 3) * 16 + 8 * (j & 1) + r8) * 16, OFF_Z2);
        const v4u sv = {SENT, SENT, SENT, SENT};
        ST128(sv, lane16, OFF_OF + (2u << 20) + ((8 * t + w) * 16 + j) * 1024);
        ST128(sv, (128 * t + 32 * rq + 4 * w + g4) * 1024 + cq * 256 + tok * 16, OFF_AA + (2u << 20));
        if (role != 1) {
          VMWAIT(0);
          BAR();
          if (tid == 0) __builtin_amdgcn_raw_buffer_store_b64((v2u){tag_lo + 1, tag_hi}, rws, (16 * t + j) * 128, FL_A + 32768, 16);
        }
      }
    }
    if (role == 1) {
      IDS
      unsigned LAS* scr = (unsigned LAS*)(sm + L_OS);
      v4u LAS* picks = (v4u LAS*)(sm + L_OS + 1024) + 8 * w;
      v4u LAS* cand = (v4u LAS*)(sm + L_OS + 2048);
      unsigned LAS* ckey = (unsigned LAS*)(sm + L_SH + 17664);
      if (tid == 0) scr[8] = 0u;
      if (tid < 64) ckey[tid] = 0u;
      v4u rc[4];
      _Pragma("unroll 1") for (unsigned spins = 0; spins < SPIN; ++spins) {
#pragma unroll
        for (int e = 0; e < 4; ++e) rc[e] = LD128(tid * 16, SM_REC + r * 32768 + e * 8192);
        const bool done = __all(max(max(umax4(rc[0]), umax4(rc[1])), max(umax4(rc[2]), umax4(rc[3]))) != SENT);
        if (r == 0 && spins == 0) {
          VMWAIT(0);
          BAR();
          if (tid == 0) __builtin_amdgcn_raw_buffer_store_b64((v2u){tag_lo + 1, tag_hi}, rws, (16 * t + j) * 128, FL_A + 32768, 16);
        }
        if (done) break;
        SLEEP(3);
      }
      unsigned m = max(max(rc[0][0], rc[1][0]), max(rc[2][0], rc[3][0]));
      m = wave_max(m);
      if (l == 0) scr[w] = m;
      BAR();
      v4u ffw[4];
#pragma unroll
      for (int i = 0; i < 4; ++i) ffw[i] = LD128(lane16, OFF_GUF + (gu_u * 16 + 2 * w) * 2048 + i * 1024);
      const v4u m0 = *(const v4u LAS*)scr, m1 = *(const v4u LAS*)(scr + 4);
      const unsigned T0 = min(min(min(m0[0], m0[1]), min(m0[2], m0[3])), min(min(m1[0], m1[1]), min(m1[2], m1[3])));
      unsigned long long bm[4];
      int tot = 0;
#pragma unroll
      for (int e = 0; e < 4; ++e) { bm[e] = __ballot(rc[e][0] >= T0); tot += __popcll(bm[e]); }
      unsigned base = 0;
      if (l == 0) base = __hip_atomic_fetch_add(scr + 8, (unsigned)tot, __ATOMIC_RELAXED, __HIP_MEMORY_SCOPE_WORKGROUP);
      base = __builtin_amdgcn_readfirstlane(base);
#pragma unroll
      for (int e = 0; e < 4; ++e) {
        const unsigned pos = base + __builtin_amdgcn_mbcnt_hi((unsigned)(bm[e] >> 32), __builtin_amdgcn_mbcnt_lo((unsigned)bm[e], 0u));
        if (rc[e][0] >= T0) { cand[pos] = rc[e]; ckey[pos] = rc[e][0]; }
        base += __popcll(bm[e]);
      }
      BAR();
      const int n = __builtin_amdgcn_readfirstlane((int)scr[8]);
      if (n <= 64) {
        const v4u mine = cand[l];
        int rank = 0;
        for (int i = 0; i < n; i += 16) {
          v4u kk[4];
#pragma unroll
          for (int e = 0; e < 4; ++e) kk[e] = *(const v4u LAS*)(ckey + i + 4 * e);
#pragma unroll
          for (int e = 0; e < 16; ++e) rank += kk[e >> 2][e & 3] > mine[0] ? 1 : 0;
        }
        if (l < n && rank < 8) picks[rank] = mine;
      } else {
        for (int c0 = 0; c0 < n; c0 += 64) {
          if (c0 + l < n) {
            const v4u mine = cand[c0 + l];
            int rank = 0;
            for (int i = 0; i < n; ++i) rank += ckey[i] > mine[0] ? 1 : 0;
            if (rank < 8) picks[rank] = mine;
          }
        }
      }
      asm volatile("s_waitcnt lgkmcnt(0)" ::: "memory");
      const v4u my = picks[l & 7];
      const int st = (0xfffe - (int)(my[0] & 0xffffu)) & 2047;
      v4u xa[2], aa[2];
      const unsigned ro = (unsigned)(st * 1024 + 128 * w + 16 * g4);
      _Pragma("unroll 1") for (unsigned spins = 0; spins < SPIN; ++spins) {
#pragma unroll
        for (int i = 0; i < 2; ++i) {
          xa[i] = r == 0 ? *(const v4u*)((const char*)X + ro + 64 * i) : LD128(ro + 64 * i, OFF_X2);
          aa[i] = LD128(ro + 64 * i, OFF_AA + r * (2u << 20));
        }
        if (spins == 0 && gu_u == 0 && w == 0) {
          ST128(my, lane16, SM_IDX + r * 1024);
          if (r == 0 && l == 0)
            __builtin_amdgcn_raw_buffer_store_b64((v2u){ep_lo + 1u, ep_hi + (ep_lo == 0xffffffffu ? 1u : 0u)}, rws, 0, FL_E, 16);
        }
        if (__all(max(umax4(aa[0]), umax4(aa[1])) != SENT)) break;
        SLEEP(2);
      }
      const float mb = __uint_as_float(my[1]), ri = __uint_as_float(my[2]);
      f32x4 ag = {0, 0, 0, 0}, au = {0, 0, 0, 0};
#pragma unroll
      for (int i = 0; i < 2; ++i) {
        v4u y;
#pragma unroll
        for (int e = 0; e < 4; ++e) y[e] = norm2(unp(pack2(unp(xa[i][e]) + unp(aa[i][e]))), mb, ri);
        ag = mfma(ffw[2 * i], y, ag);
        au = mfma(ffw[2 * i + 1], y, au);
      }
      f32x4 LAS* red = (f32x4 LAS*)(sm + L_SH);
      if (tok < 8) { red[w * 64 + g4 * 8 + tok] = ag; red[w * 64 + 32 + g4 * 8 + tok] = au; }
      BAR();
      if (w == 0) {
        const f32x2 LAS* rd = (const f32x2 LAS*)red + l;
        f32x2 gt = rd[0], up = rd[64];
#pragma unroll
        for (int i = 1; i < 8; ++i) { gt += rd[i * 128]; up += rd[i * 128 + 64]; }
        const f32x2 gv = unp(pack2(gt)), uv = unp(pack2(up));
        f32x2 sg;
#pragma unroll
        for (int e = 0; e < 2; ++e) sg[e] = gv[e] * __builtin_amdgcn_rcpf(1.0f + __expf(-gv[e]));
        const unsigned o = pack2(unp(pack2(sg)) * uv);
        __builtin_amdgcn_raw_buffer_store_b32(o, rws, gu_u * 256 + (l >> 5) * 128 + ((l >> 1) & 7) * 16 + ((l >> 4) & 1) * 8 + (l & 1) * 4,
                                              SM_HF + r * 32768, 16);
      }
      BAR();
    }
    if (r == 0) qkv_gemm<1>(sm, rws, w0, tag_lo, tag_hi, Wg, Wu, Wd);
    v4u srec;
    {
      IDS
      v4u LAS* scr = (v4u LAS*)(sm + L_SH + 32000);
      if (w == 7) {
        v4u f;
        _Pragma("unroll 1") for (unsigned spins = 0; spins < SPIN; ++spins) {
          f = LD128(xcd * 128 + (l & 7) * 16, SM_IDX + r * 1024);
          if (__all(valid4(f))) break;
          SLEEP(4);
        }
        if (l < 8) scr[l] = f;
      }
      BAR();
      srec = scr[l & 7];
    }
    const int stok = (0xfffe - (int)(srec[0] & 0xffffu)) & 2047;
    if (role == 2) {
      IDS
      v4u ffw[7];
#pragma unroll
      for (int i = 0; i < 7; ++i) ffw[i] = LD128(lane16, OFF_DNF + (dn_u * 56 + 7 * w + i) * 1024);
      v2u xa, aa;
      const unsigned ro = (unsigned)(stok * 1024 + dn_u * 32 + 8 * g4);
      _Pragma("unroll 1") for (unsigned spins = 0; spins < SPIN; ++spins) {
        xa = r == 0 ? *(const v2u*)((const char*)X + ro) : __builtin_amdgcn_raw_buffer_load_b64(rws, ro, OFF_X2, 16);
        aa = __builtin_amdgcn_raw_buffer_load_b64(rws, ro, OFF_AA + r * (2u << 20), 16);
        if (__all(max(aa[0], aa[1]) != SENT)) break;
        SLEEP(2);
      }
      v4u hf[7];
      _Pragma("unroll 1") for (unsigned spins = 0; spins < SPIN; ++spins) {
        unsigned mx = 0;
#pragma unroll
        for (int s = 0; s < 7; ++s) {
          hf[s] = LD128((7 * w + s) * 512 + g4 * 128 + (l & 7) * 16, SM_HF + r * 32768);
          mx = max(mx, umax4(hf[s]));
        }
        if (__all(mx != SENT)) break;
        SLEEP(3);
      }
      f32x4 acc = {0, 0, 0, 0};
#pragma unroll
      for (int s = 0; s < 7; ++s) acc = mfma(ffw[s], hf[s], acc);
      f32x4 LAS* red = (f32x4 LAS*)(sm + L_OS + 16384);
      red[w * 64 + l] = acc;
      BAR();
      if (w == 0 && tok < 8) {
        f32x4 y = red[l];
#pragma unroll
        for (int i = 1; i < 8; ++i) y += red[i * 64 + l];
        unsigned u = srec[0] >> 16;
        u = (u & 0x8000u) ? (u ^ 0x8000u) : (u ^ 0xffffu);
        const float rr = __uint_as_float(u << 16);
        const float gt = rbf(__builtin_amdgcn_rcpf(1.0f + __expf(-rr)));
        const float mb = __uint_as_float(srec[1]), ri = __uint_as_float(srec[2]);
        v2u o;
#pragma unroll
        for (int e = 0; e < 2; ++e) {
          const f32x2 x1 = unp(pack2(unp(xa[e]) + unp(aa[e])));
          const f32x2 y1 = unp(norm2(x1, mb, ri));
          const f32x2 up = unp(pack2(unp(pack2(y[2 * e], y[2 * e + 1])) * gt));
          o[e] = pack2(x1 + unp(pack2(y1 + up)));
        }
        if (r == 0) __builtin_amdgcn_raw_buffer_store_b64(o, rws, tok * 1024 + dn_u * 32 + 8 * g4, SM_X2S, 16);
        else *(v2u*)((char*)Out + ro) = o;
      }
      BAR();
    }
    {
      IDS
      const unsigned long long own = __ballot(stok == myrow);
      if (r == 0) {
        const int lo = 128 * t - 16;
        const bool inr = stok >= lo && stok < lo + 144;
        const unsigned inmask = (unsigned)__ballot(inr) & 0xffu;
        if (inmask) {
          if ((inmask >> w) & 1) {
            v4u xv;
            _Pragma("unroll 1") for (unsigned spins = 0; spins < SPIN; ++spins) {
              xv = LD128(lane16, SM_X2S + w * 1024);
              if (__all(valid4(xv))) break;
              SLEEP(3);
            }
            f32x2 x[4] = {unp(xv[0]), unp(xv[1]), unp(xv[2]), unp(xv[3])};
            float mb, ri;
            *(v4u LAS*)(sm + L_OS + l * 128 + w * 16) = ln_row(x, mb, ri);
          }
          BAR();
          if (w < 6) {
            f32x4 acc = {0, 0, 0, 0};
#pragma unroll
            for (int s = 0; s < 16; ++s)
              acc = mfma(*(const v4u LAS*)(sm + L_W + (6 * s + w) * 1024 + lane16),
                         *(const v4u LAS*)(sm + L_OS + (4 * s + g4) * 128 + (l & 7) * 16), acc);
            if (inr && tok < 8)
              *(v2u LAS*)(sm + L_SH + (2 * w + (g4 >> 1)) * PLANE + 8 * (g4 & 1) + (stok - lo) * 16) = packacc(acc);
          }
          if (own) {
            const int slot = __builtin_ctzll(own);
            _Pragma("unroll 1") for (unsigned spins = 0; spins < SPIN; ++spins) {
              xres = LD128(lane16, SM_X2S + slot * 1024);
              if (__all(valid4(xres))) break;
              SLEEP(2);
            }
            ST128(xres, lane16, OFF_X2 + myrow * 1024);
          }
          BAR();
        }
        attention(sm, rws, w0, 1);
      } else if (!own) {
        *(v4u*)((char*)Out + myrow * 1024 + lane16) = xo;
      }
    }
  }
}

extern "C" void kernel_launch(void* const* d_in, const int* in_sizes, int n_in, void* d_out, int out_size,
                              void* d_ws, size_t ws_size, hipStream_t stream) {
  (void)hipFuncSetAttribute((const void*)k_main, hipFuncAttributeMaxDynamicSharedMemorySize, L_TOTAL);
  k_main<<<256, 512, L_TOTAL, stream>>>((const u16*)d_in[0], (const u16*)d_in[1], (const u16*)d_in[2], (const u16*)d_in[4],
                                        (const u16*)d_in[5], (const u16*)d_in[6], (const u16*)d_in[7], (u16*)d_out, (char*)d_ws);
}
```
